```python
import torch

def _softmax(x, axis=-1):
    return torch.softmax(x, dim=axis)
def _gelu(x):
    return torch.nn.functional.gelu(x, approximate="tanh")
def _silu(x):
    return torch.nn.functional.silu(x)
def _sigmoid(x):
    return torch.sigmoid(x)
def _layernorm(x, axis=-1, eps=1e-5):
    m = x.mean(dim=axis, keepdim=True)
    v = x.var(dim=axis, keepdim=True, unbiased=False)
    return (x - m) / torch.sqrt(v + eps)
def _scatter_add(tgt, idx, src, axis=0):
    return tgt.clone().index_add_(axis, idx, src)
def _segment_sum(src, seg, n):
    out = torch.zeros((n,) + src.shape[1:], dtype=src.dtype)
    return out.index_add_(0, seg, src)
def _pad(x, pw):
    flat = tuple(v for pair in reversed(pw) for v in pair)
    return torch.nn.functional.pad(x, flat)

kernel_name = 'sequential_preLN__mha_block_cached__dense_activation_squared_relu__decode'

def setup_inputs():

    _w = (lambda a: (lambda t: t.to(torch.bfloat16) if t.is_floating_point() else t)(torch.as_tensor(a)))
    def _A_setup():
        B, Sq, D = 1, 1, 448
        H, Sk, Dh = 16, 32768, 256
        return {
            "x": _w((lambda *s: torch.randn(*s, dtype=torch.bfloat16) * 0.05)(B, Sq, D)),
            "k_cache": _w((lambda *s: torch.randn(*s, dtype=torch.bfloat16) * 3.2)(B, H, Sk, Dh)),
            "v_cache": _w((lambda *s: torch.randn(*s, dtype=torch.bfloat16) * 0.4)(B, H, Sk, Dh)),
            "wq": _w((lambda *s: torch.randn(*s, dtype=torch.bfloat16) * 3.2)(D, H * Dh) * (D ** -0.5)),
            "wo": _w((lambda *s: torch.randn(*s, dtype=torch.bfloat16) * 0.4)(H * Dh, D) * ((H * Dh) ** -0.5)),
        }
    _A_in = _A_setup()

    def _B_setup():
        B, S, D, F = 1, 1, 448, 1792
        return {
            "x": _w((lambda *s: torch.randn(*s, dtype=torch.bfloat16) * 0.05)(B, S, D)),
            "w1": _w((lambda *s: torch.randn(*s, dtype=torch.bfloat16) * 0.05)(D, F)),
            "b1": _w((lambda *s: torch.randn(*s, dtype=torch.bfloat16) * 0.8)(F)),
            "w2": _w((lambda *s: torch.randn(*s, dtype=torch.bfloat16) * 0.05)(F, D)),
            "b2": _w((lambda *s: torch.randn(*s, dtype=torch.bfloat16) * 3.2)(D)),
        }
    _B_in = _B_setup()

    return {
        "A_x": _A_in["x"],
        "A_k_cache": _A_in["k_cache"],
        "A_v_cache": _A_in["v_cache"],
        "A_wq": _A_in["wq"],
        "A_wo": _A_in["wo"],
        "B_x": _B_in["x"],
        "B_w1": _B_in["w1"],
        "B_b1": _B_in["b1"],
        "B_w2": _B_in["w2"],
        "B_b2": _B_in["b2"],
    }

def reference(A_x, A_k_cache, A_v_cache, A_wq, A_wo, B_x, B_w1, B_b1, B_w2, B_b2):
    def _A_ref(x, k_cache, v_cache, wq, wo):
        B, Sq, D = x.shape
        H, Dh = 16, 256
        q = (lambda x, *d: x.permute(*d))((x @ wq).reshape(B, Sq, H, Dh), 0, 2, 1, 3)
        scores = torch.einsum("bhqd,bhkd->bhqk", q, k_cache) * (Dh ** -0.5)
        p = _softmax(scores, axis=-1)
        o = torch.einsum("bhqk,bhkd->bhqd", p, v_cache)
        return (lambda x, *d: x.permute(*d))(o, 0, 2, 1, 3).reshape(B, Sq, H * Dh) @ wo

    def _B_ref(x, w1, b1, w2, b2):
        h = torch.einsum("bsd,df->bsf", x, w1) + b1
        zero = torch.zeros_like(h)
        r = torch.where(h > zero, h, zero)
        h = r * r
        out = torch.einsum("bsf,fd->bsd", h, w2) + b2
        return out

    x = A_x
    h = x + _A_ref(_layernorm(x, axis=-1), A_k_cache, A_v_cache, A_wq, A_wo)
    h2 = h + _B_ref(_layernorm(h, axis=-1), B_w1, B_b1, B_w2, B_b2)
    return h2
```

```cpp
#include <hip/hip_runtime.h>
#include <hip/hip_bf16.h>
#include <stdint.h>

namespace {
constexpr int D = 448, H = 16, SK = 32768, DH = 256, F = 1792;
constexpr int NSPLIT = 16, WAVES = 8, KW = SK / NSPLIT / WAVES;
constexpr int NGRP = KW / 16;

typedef unsigned v4u __attribute__((ext_vector_type(4)));
typedef float v4f __attribute__((ext_vector_type(4)));
typedef __bf16 bf16x2 __attribute__((ext_vector_type(2)));
typedef unsigned short u16;

__device__ __forceinline__ float bf16r(float x) { return (float)(__bf16)x; }
__device__ __forceinline__ float bf2f(u16 v) { return __builtin_bit_cast(float, (unsigned)v << 16); }
__device__ __forceinline__ float lo16(unsigned v) { return __builtin_bit_cast(float, v << 16); }
__device__ __forceinline__ float hi16(unsigned v) { return __builtin_bit_cast(float, v & 0xffff0000u); }
__device__ __forceinline__ unsigned packbf(float lo, float hi) {
  return (__builtin_bit_cast(unsigned, lo) >> 16) | (__builtin_bit_cast(unsigned, hi) & 0xffff0000u);
}
__device__ __forceinline__ float dot2(unsigned k, unsigned q, float acc) {
  return __builtin_amdgcn_fdot2_f32_bf16(__builtin_bit_cast(bf16x2, k), __builtin_bit_cast(bf16x2, q), acc, false);
}
template <int CTRL> __device__ __forceinline__ float dpp(float x) {
  return __builtin_bit_cast(float, __builtin_amdgcn_mov_dpp(__builtin_bit_cast(int, x), CTRL, 0xf, 0xf, true));
}
constexpr int XOR1 = 0xB1, XOR2 = 0x4E, XOR7 = 0x141, XOR8 = 0x128;
__device__ __forceinline__ float swap16_add(float a, float b) {
  auto s = __builtin_amdgcn_permlane16_swap(__builtin_bit_cast(unsigned, a), __builtin_bit_cast(unsigned, b), false, false);
  return __builtin_bit_cast(float, (unsigned)s[0]) + __builtin_bit_cast(float, (unsigned)s[1]);
}
__device__ __forceinline__ float swap32_add(float a) {
  auto s = __builtin_amdgcn_permlane32_swap(__builtin_bit_cast(unsigned, a), __builtin_bit_cast(unsigned, a), false, false);
  return __builtin_bit_cast(float, (unsigned)s[0]) + __builtin_bit_cast(float, (unsigned)s[1]);
}
__device__ __forceinline__ float wave_sum(float v) {
  v += dpp<XOR1>(v); v += dpp<XOR2>(v); v += dpp<XOR7>(v); v += dpp<XOR8>(v);
  v = swap16_add(v, v);
  return swap32_add(v);
}
__device__ __forceinline__ float wave_max(float v) {
  v = fmaxf(v, dpp<XOR1>(v)); v = fmaxf(v, dpp<XOR2>(v)); v = fmaxf(v, dpp<XOR7>(v)); v = fmaxf(v, dpp<XOR8>(v));
  auto s = __builtin_amdgcn_permlane16_swap(__builtin_bit_cast(unsigned, v), __builtin_bit_cast(unsigned, v), false, false);
  v = fmaxf(__builtin_bit_cast(float, (unsigned)s[0]), __builtin_bit_cast(float, (unsigned)s[1]));
  auto t = __builtin_amdgcn_permlane32_swap(__builtin_bit_cast(unsigned, v), __builtin_bit_cast(unsigned, v), false, false);
  return fmaxf(__builtin_bit_cast(float, (unsigned)t[0]), __builtin_bit_cast(float, (unsigned)t[1]));
}
__device__ __forceinline__ void layernorm7(const float (&x)[7], float (&y)[7]) {
  float s = 0.f;
#pragma unroll
  for (int j = 0; j < 7; ++j) s += x[j];
  const float mean = wave_sum(s) * (1.f / D);
  float v = 0.f;
#pragma unroll
  for (int j = 0; j < 7; ++j) { const float d = x[j] - mean; v = fmaf(d, d, v); }
  const float var = bf16r(wave_sum(v) * (1.f / D));
  const float mb = bf16r(mean);
  const float sd = bf16r(sqrtf(bf16r(var + 1e-5f)));
#pragma unroll
  for (int j = 0; j < 7; ++j) y[j] = bf16r(bf16r(x[j] - mb) / sd);
}

__global__ void __launch_bounds__(WAVES * 64) attn_kernel(const u16* __restrict__ x, const char* __restrict__ Kc, const char* __restrict__ Vc,
                                                          const u16* __restrict__ wq, float* __restrict__ pml, float* __restrict__ po) {
  __shared__ __attribute__((aligned(16))) float lds[448 + WAVES * 264 + WAVES * KW];
  float* xn = lds;
  float* red = lds + 448;
  float* sc = red + WAVES * 264;
  const int lane = threadIdx.x & 63, wave = __builtin_amdgcn_readfirstlane(threadIdx.x >> 6);
  const int hf = lane >> 5, c = lane & 31;
  const int b = (blockIdx.x & 7) * 32 + (blockIdx.x >> 3);
  const int h = b >> 4, s = b & 15;
  const size_t wbase = ((size_t)h * SK + (size_t)s * (SK / NSPLIT) + (size_t)wave * KW) * (DH * 2);
  const char* Kw = Kc + wbase;
  const char* Vw = Vc + wbase;
  const int r0 = (b * 37 + wave * 5) & (2 * NGRP - 1);
  const unsigned loff = lane * 16;
  auto ld4 = [&](const char* base, int sub, v4u* dst) {
    const char* p = base + (size_t)((sub + r0) & (2 * NGRP - 1)) * 4096 + loff;
#pragma unroll
    for (int i = 0; i < 4; ++i) dst[i] = __builtin_nontemporal_load((const v4u*)(p + i * 1024));
  };
  v4u A[8], B[8];
  constexpr int QI = 224 / WAVES;
  v4u wv[QI];
  {
    const u16* wp = wq + (size_t)(2 * QI * wave + hf) * (H * DH) + h * DH + c * 8;
#pragma unroll
    for (int i = 0; i < QI; ++i) wv[i] = *(const v4u*)(wp + (size_t)2 * i * (H * DH));
  }
  ld4(Kw, 0, A); ld4(Kw, 1, A + 4);
  if (wave == 0) {
    float xv[7], y[7];
#pragma unroll
    for (int j = 0; j < 7; ++j) xv[j] = bf2f(x[lane + 64 * j]);
    layernorm7(xv, y);
#pragma unroll
    for (int j = 0; j < 7; ++j) { const int e = lane + 64 * j; xn[(e & 1) * 224 + (e >> 1)] = y[j]; }
  }
  __syncthreads();
  float acc[8];
#pragma unroll
  for (int k = 0; k < 8; ++k) acc[k] = 0.f;
#pragma unroll
  for (int i = 0; i < QI; ++i) {
    const float xr = xn[hf * 224 + QI * wave + i];
#pragma unroll
    for (int k = 0; k < 4; ++k) { acc[2 * k] = fmaf(xr, lo16(wv[i][k]), acc[2 * k]); acc[2 * k + 1] = fmaf(xr, hi16(wv[i][k]), acc[2 * k + 1]); }
  }
#pragma unroll
  for (int k = 0; k < 8; ++k) acc[k] = swap32_add(acc[k]);
  if (hf == 0) {
    *(v4f*)(red + wave * 264 + c * 8) = v4f{acc[0], acc[1], acc[2], acc[3]};
    *(v4f*)(red + wave * 264 + c * 8 + 4) = v4f{acc[4], acc[5], acc[6], acc[7]};
  }
  __syncthreads();
  v4u q;
  {
    v4f a = {0, 0, 0, 0}, bb = {0, 0, 0, 0};
#pragma unroll
    for (int w = 0; w < WAVES; ++w) { a += *(const v4f*)(red + w * 264 + c * 8); bb += *(const v4f*)(red + w * 264 + c * 8 + 4); }
    q[0] = packbf(bf16r(a[0]), bf16r(a[1])); q[1] = packbf(bf16r(a[2]), bf16r(a[3]));
    q[2] = packbf(bf16r(bb[0]), bf16r(bb[1])); q[3] = packbf(bf16r(bb[2]), bf16r(bb[3]));
  }
  float* scw = sc + wave * KW;
  const bool b3 = lane & 8, b2 = lane & 4;
  const int vidx = ((lane >> 4) & 1) * 4 + ((lane >> 3) & 1) * 2 + ((lane >> 2) & 1);
  auto qk = [&](const v4u (&T)[8], int g) {
    float xx[8];
#pragma unroll
    for (int i = 0; i < 8; ++i) { float d = dot2(T[i][0], q[0], 0.f); d = dot2(T[i][1], q[1], d); d = dot2(T[i][2], q[2], d); xx[i] = dot2(T[i][3], q[3], d); }
    float y[4];
#pragma unroll
    for (int i = 0; i < 4; ++i) y[i] = swap16_add(xx[i], xx[i + 4]);
    float z[2];
#pragma unroll
    for (int i = 0; i < 2; ++i) { const float t = b3 ? y[i + 2] : y[i], u = b3 ? y[i] : y[i + 2]; z[i] = t + dpp<XOR8>(u); }
    const float t = b2 ? z[1] : z[0], u = b2 ? z[0] : z[1];
    float w = t + dpp<XOR7>(u);
    w += dpp<XOR2>(w); w += dpp<XOR1>(w);
    scw[hf * (KW / 2) + g * 8 + vidx] = bf16r(w) * 0.0625f;
  };
  auto pv = [&](const v4u (&T)[8], int g) {
    const v4f p0 = *(const v4f*)(scw + hf * (KW / 2) + g * 8), p1 = *(const v4f*)(scw + hf * (KW / 2) + g * 8 + 4);
#pragma unroll
    for (int i = 0; i < 8; ++i) {
      const float p = i < 4 ? p0[i & 3] : p1[i & 3];
#pragma unroll
      for (int k = 0; k < 4; ++k) { acc[2 * k] = fmaf(p, lo16(T[i][k]), acc[2 * k]); acc[2 * k + 1] = fmaf(p, hi16(T[i][k]), acc[2 * k + 1]); }
    }
  };
  for (int g = 0; g < NGRP; g += 2) {
    ld4(Kw, 2 * g + 2, B); ld4(Kw, 2 * g + 3, B + 4);
    __builtin_amdgcn_sched_barrier(0);
    qk(A, g);
    __builtin_amdgcn_sched_barrier(0);
    if (g + 2 < NGRP) { ld4(Kw, 2 * g + 4, A); ld4(Kw, 2 * g + 5, A + 4); } else { ld4(Vw, 0, A); ld4(Vw, 1, A + 4); }
    __builtin_amdgcn_sched_barrier(0);
    qk(B, g + 1);
    __builtin_amdgcn_sched_barrier(0);
  }
  float m, l;
  {
    v4f sv = *(const v4f*)(scw + lane * 4);
    m = wave_max(fmaxf(fmaxf(sv[0], sv[1]), fmaxf(sv[2], sv[3])));
    constexpr float L2E = 1.4426950408889634f;
#pragma unroll
    for (int k = 0; k < 4; ++k) sv[k] = __builtin_amdgcn_exp2f((sv[k] - m) * L2E);
    l = wave_sum((sv[0] + sv[1]) + (sv[2] + sv[3]));
    *(v4f*)(scw + lane * 4) = sv;
  }
#pragma unroll
  for (int k = 0; k < 8; ++k) acc[k] = 0.f;
  for (int g = 0; g < NGRP; g += 2) {
    ld4(Vw, 2 * g + 2, B); ld4(Vw, 2 * g + 3, B + 4);
    __builtin_amdgcn_sched_barrier(0);
    pv(A, g);
    __builtin_amdgcn_sched_barrier(0);
    if (g + 2 < NGRP) { ld4(Vw, 2 * g + 4, A); ld4(Vw, 2 * g + 5, A + 4); }
    __builtin_amdgcn_sched_barrier(0);
    pv(B, g + 1);
    __builtin_amdgcn_sched_barrier(0);
  }
#pragma unroll
  for (int k = 0; k < 8; ++k) acc[k] = swap32_add(acc[k]);
  if (hf == 0) {
    *(v4f*)(red + wave * 264 + c * 8) = v4f{acc[0], acc[1], acc[2], acc[3]};
    *(v4f*)(red + wave * 264 + c * 8 + 4) = v4f{acc[4], acc[5], acc[6], acc[7]};
  }
  if (lane == 0) { red[wave * 264 + 256] = m; red[wave * 264 + 257] = l; }
  __syncthreads();
  if (threadIdx.x < 256) {
    float M = red[256];
#pragma unroll
    for (int w = 1; w < WAVES; ++w) M = fmaxf(M, red[w * 264 + 256]);
    float o = 0.f, L = 0.f;
#pragma unroll
    for (int w = 0; w < WAVES; ++w) { const float sc_ = __builtin_amdgcn_exp2f((red[w * 264 + 256] - M) * 1.4426950408889634f); o = fmaf(sc_, red[w * 264 + threadIdx.x], o); L = fmaf(sc_, red[w * 264 + 257], L); }
    po[(size_t)b * 256 + threadIdx.x] = o;
    if (threadIdx.x == 0) { pml[2 * b] = M; pml[2 * b + 1] = L; }
  }
}

__global__ void __launch_bounds__(256) k_wo(const float* __restrict__ pml, const float* __restrict__ po, const u16* __restrict__ wo, float* __restrict__ yp) {
  __shared__ float o[256];
  __shared__ float red[256];
  const int h = blockIdx.x / 7, cc = blockIdx.x % 7, t = threadIdx.x;
  float M = -INFINITY;
  for (int s = 0; s < NSPLIT; ++s) M = fmaxf(M, pml[2 * (h * 16 + s)]);
  float L = 0.f, a = 0.f;
  for (int s = 0; s < NSPLIT; ++s) { const float w = __builtin_amdgcn_exp2f((pml[2 * (h * 16 + s)] - M) * 1.4426950408889634f); L = fmaf(w, pml[2 * (h * 16 + s) + 1], L); a = fmaf(w, po[(size_t)(h * 16 + s) * 256 + t], a); }
  o[t] = bf16r(a / L);
  __syncthreads();
  const int n = t & 63, dq = t >> 6;
  float y = 0.f;
  for (int d = dq * 64; d < dq * 64 + 64; ++d) y = fmaf(o[d], bf2f(wo[(size_t)(h * 256 + d) * D + cc * 64 + n]), y);
  red[t] = y;
  __syncthreads();
  if (t < 64) yp[h * D + cc * 64 + t] = (red[t] + red[t + 64]) + (red[t + 128] + red[t + 192]);
}

__global__ void __launch_bounds__(256) k_w1(const u16* __restrict__ x, const float* __restrict__ yp, const u16* __restrict__ w1, const u16* __restrict__ b1, float* __restrict__ hbuf, float* __restrict__ h1) {
  __shared__ float xn[D];
  __shared__ float red[256];
  const int t = threadIdx.x;
  if (t < 64) {
    float hv[7], y[7];
    for (int j = 0; j < 7; ++j) { const int e = t + 64 * j; float a = 0.f; for (int h = 0; h < H; ++h) a += yp[h * D + e]; hv[j] = bf16r(bf2f(x[e]) + bf16r(a)); if (blockIdx.x == 0) hbuf[e] = hv[j]; }
    layernorm7(hv, y);
    for (int j = 0; j < 7; ++j) xn[t + 64 * j] = y[j];
  }
  __syncthreads();
  const int n = t & 63, kq = t >> 6, col = blockIdx.x * 64 + n;
  float a = 0.f;
  for (int k = kq * 112; k < kq * 112 + 112; ++k) a = fmaf(xn[k], bf2f(w1[(size_t)k * F + col]), a);
  red[t] = a;
  __syncthreads();
  if (t < 64) {
    float v = bf16r((red[t] + red[t + 64]) + (red[t + 128] + red[t + 192]));
    v = bf16r(v + bf2f(b1[col]));
    v = v > 0.f ? v : 0.f;
    h1[col] = bf16r(v * v);
  }
}

__global__ void __launch_bounds__(1024) k_w2(const float* __restrict__ hbuf, const float* __restrict__ h1, const u16* __restrict__ w2, const u16* __restrict__ b2, __hip_bfloat16* __restrict__ out) {
  __shared__ float red[1024];
  const int t = threadIdx.x, n = t & 63, kq = t >> 6, col = blockIdx.x * 64 + n;
  float a = 0.f;
  for (int k = kq * 112; k < kq * 112 + 112; ++k) a = fmaf(h1[k], bf2f(w2[(size_t)k * D + col]), a);
  red[t] = a;
  __syncthreads();
  if (t < 64) {
    float v = 0.f;
    for (int i = 0; i < 16; ++i) v += red[t + 64 * i];
    v = bf16r(bf16r(v) + bf2f(b2[col]));
    v = bf16r(hbuf[col] + v);
    ((u16*)out)[col] = (u16)(__builtin_bit_cast(unsigned, v) >> 16);
  }
}
}

extern "C" void kernel_launch(void* const* d_in, const int* in_sizes, int n_in, void* d_out, int out_size, void* d_ws, size_t ws_size, hipStream_t stream) {
  const u16* x = (const u16*)d_in[0];
  const char* Kc = (const char*)d_in[1];
  const char* Vc = (const char*)d_in[2];
  const u16* wq = (const u16*)d_in[3];
  const u16* wo = (const u16*)d_in[4];
  const u16* w1 = (const u16*)d_in[6];
  const u16* b1 = (const u16*)d_in[7];
  const u16* w2 = (const u16*)d_in[8];
  const u16* b2 = (const u16*)d_in[9];
  float* ws = (float*)d_ws;
  float* pml = ws;
  float* po = ws + 1024;
  float* yp = po + 65536;
  float* hbuf = yp + 8192;
  float* h1 = hbuf + 512;
  attn_kernel<<<256, WAVES * 64, 0, stream>>>(x, Kc, Vc, wq, pml, po);
  k_wo<<<16 * 7, 256, 0, stream>>>(pml, po, wo, yp);
  k_w1<<<28, 256, 0, stream>>>(x, yp, w1, b1, hbuf, h1);
  k_w2<<<7, 1024, 0, stream>>>(hbuf, h1, w2, b2, (__hip_bfloat16*)d_out);
}
```

```cpp
#include <hip/hip_runtime.h>
#include <hip/hip_bf16.h>
#include <stdint.h>

namespace {
constexpr int D = 448, H = 16, SK = 32768, DH = 256, F = 1792;
constexpr int NSPLIT = 16, WAVES = 8, KW = SK / NSPLIT / WAVES;
constexpr int NGRP = KW / 16;
constexpr int NMLP = 28, NFIN = 7;
constexpr float L2E = 1.4426950408889634f;
constexpr unsigned SENT = 0xFFFFFFFFu;
constexpr unsigned SPIN_CAP = 1u << 16;
constexpr int AUX_SC1 = 16;

constexpr int WS_EPOCH = 0;
constexpr int WS_FLAG = 256;
constexpr int WS_PML = 4096;
constexpr int WS_Y = 8192;
constexpr int WS_OP = WS_Y + 16 * 448 * 4;
constexpr int WS_PO = 131072;
constexpr int WS_BYTES = WS_PO + 256 * 1024;
static_assert(WS_OP + NMLP * 448 * 4 <= WS_PO, "ws map");

constexpr int L_XN = 0;
constexpr int L_RED = L_XN + WAVES * 448;
constexpr int L_RED2 = L_RED + WAVES * 264;
constexpr int L_SC = L_RED2 + WAVES * 264;
constexpr int L_Y = L_SC + WAVES * KW;
constexpr int L_H = L_Y + WAVES * 32;
constexpr int L_H1 = L_H + 448;
constexpr int L_OP = L_H1 + WAVES * 64;
constexpr int L_END = L_OP + WAVES * 448;
constexpr int LDS_BYTES = 96 * 1024;
static_assert(L_END * 4 <= LDS_BYTES, "lds map");

typedef unsigned v4u __attribute__((ext_vector_type(4)));
typedef unsigned v2u __attribute__((ext_vector_type(2)));
typedef float v4f __attribute__((ext_vector_type(4)));
typedef float v2f __attribute__((ext_vector_type(2)));
typedef __bf16 bf16x2 __attribute__((ext_vector_type(2)));
typedef unsigned short u16;
typedef unsigned long long u64;
#define GAS __attribute__((address_space(1)))
#define LAS __attribute__((address_space(3)))

__device__ __forceinline__ float asf(unsigned v) { return __builtin_bit_cast(float, v); }
__device__ __forceinline__ unsigned asu(float v) { return __builtin_bit_cast(unsigned, v); }
__device__ __forceinline__ float bf16r(float x) { return (float)(__bf16)x; }
__device__ __forceinline__ float bf2f(u16 v) { return asf((unsigned)v << 16); }
__device__ __forceinline__ float lo16(unsigned v) { return asf(v << 16); }
__device__ __forceinline__ float hi16(unsigned v) { return asf(v & 0xffff0000u); }
__device__ __forceinline__ unsigned packbf(float lo, float hi) { return (asu(lo) >> 16) | (asu(hi) & 0xffff0000u); }
__device__ __forceinline__ unsigned perm_lo(unsigned a, unsigned b) { return __builtin_amdgcn_perm(b, a, 0x05040100u); }
__device__ __forceinline__ unsigned perm_hi(unsigned a, unsigned b) { return __builtin_amdgcn_perm(b, a, 0x07060302u); }
__device__ __forceinline__ float dot2(unsigned k, unsigned q, float acc) {
  return __builtin_amdgcn_fdot2_f32_bf16(__builtin_bit_cast(bf16x2, k), __builtin_bit_cast(bf16x2, q), acc, false);
}
template <int CTRL> __device__ __forceinline__ float dpp(float x) {
  return asf(__builtin_amdgcn_mov_dpp(__builtin_bit_cast(int, x), CTRL, 0xf, 0xf, true));
}
constexpr int XOR1 = 0xB1, XOR2 = 0x4E, XOR7 = 0x141, XOR8 = 0x128;
__device__ __forceinline__ float swap16_add(float a, float b) {
  auto s = __builtin_amdgcn_permlane16_swap(asu(a), asu(b), false, false);
  return asf(s[0]) + asf(s[1]);
}
__device__ __forceinline__ float swap32_add(float a, float b) {
  auto s = __builtin_amdgcn_permlane32_swap(asu(a), asu(b), false, false);
  return asf(s[0]) + asf(s[1]);
}
__device__ __forceinline__ float swap16_max(float a) {
  auto s = __builtin_amdgcn_permlane16_swap(asu(a), asu(a), false, false);
  return fmaxf(asf(s[0]), asf(s[1]));
}
__device__ __forceinline__ float swap32_max(float a) {
  auto s = __builtin_amdgcn_permlane32_swap(asu(a), asu(a), false, false);
  return fmaxf(asf(s[0]), asf(s[1]));
}
__device__ __forceinline__ float sum_hi3(float v) { v += dpp<XOR8>(v); v = swap16_add(v, v); return swap32_add(v, v); }
__device__ __forceinline__ float max_hi3(float v) { v = fmaxf(v, dpp<XOR8>(v)); return swap32_max(swap16_max(v)); }
__device__ __forceinline__ float sum_lo3(float v) { v += dpp<XOR1>(v); v += dpp<XOR2>(v); return v + dpp<XOR7>(v); }
__device__ __forceinline__ float wave_sum(float v) { return sum_hi3(sum_lo3(v)); }
__device__ __forceinline__ float wave_max(float v) {
  v = fmaxf(v, dpp<XOR1>(v)); v = fmaxf(v, dpp<XOR2>(v)); v = fmaxf(v, dpp<XOR7>(v));
  return max_hi3(v);
}
__device__ __forceinline__ void ln_stats(const float (&x)[7], float& mb, float& sd) {
  float s = 0.f;
#pragma unroll
  for (int j = 0; j < 7; ++j) s += x[j];
  const float mean = wave_sum(s) * (1.f / D);
  float v = 0.f;
#pragma unroll
  for (int j = 0; j < 7; ++j) { const float d = x[j] - mean; v = fmaf(d, d, v); }
  const float var = bf16r(wave_sum(v) * (1.f / D));
  mb = bf16r(mean);
  sd = bf16r(sqrtf(bf16r(var + 1e-5f)));
}
__device__ __forceinline__ float ln_apply(float x, float mb, float sd) { return bf16r(bf16r(x - mb) / sd); }

__device__ __forceinline__ unsigned nosent(float v) { return min(asu(v), 0xFFFFFFFEu); }
__device__ __forceinline__ v4u nosent4(v4f v) { return v4u{nosent(v[0]), nosent(v[1]), nosent(v[2]), nosent(v[3])}; }
__device__ __forceinline__ bool full4(v4u v) { return v[0] != SENT && v[1] != SENT && v[2] != SENT && v[3] != SENT; }
__device__ __forceinline__ v4f asf4(v4u v) { return __builtin_bit_cast(v4f, v); }
__device__ __forceinline__ v4u ld128(__amdgpu_buffer_rsrc_t rs, int off) { return __builtin_amdgcn_raw_buffer_load_b128(rs, off, 0, AUX_SC1); }
__device__ __forceinline__ v2u ld64(__amdgpu_buffer_rsrc_t rs, int off) { return __builtin_amdgcn_raw_buffer_load_b64(rs, off, 0, AUX_SC1); }
__device__ __forceinline__ void st128(__amdgpu_buffer_rsrc_t rs, int off, v4u v) { __builtin_amdgcn_raw_buffer_store_b128(v, rs, off, 0, AUX_SC1); }
__device__ __forceinline__ void st64(__amdgpu_buffer_rsrc_t rs, int off, v2u v) { __builtin_amdgcn_raw_buffer_store_b64(v, rs, off, 0, AUX_SC1); }
#define DRAIN() asm volatile("s_waitcnt vmcnt(0)" ::: "memory")
#define CBAR() asm volatile("" ::: "memory")
#define SCHED() __builtin_amdgcn_sched_barrier(0)

struct Args {
  const u16* x; const char* Kc; const char* Vc; const u16* wq; const u16* wo;
  const u16* w1; const u16* b1; const u16* w2; const u16* b2; u16* out; char* ws;
};

__global__ void __launch_bounds__(WAVES * 64, 2) fused_kernel(Args a) {
  extern __shared__ __attribute__((aligned(16))) float lds[];
  const int lane = threadIdx.x & 63, wave = __builtin_amdgcn_readfirstlane(threadIdx.x >> 6);
  const int hf = lane >> 5, c = lane & 31;
  const int b = (blockIdx.x & 7) * 32 + (blockIdx.x >> 3);
  const int h = b >> 4, s = b & 15;
  const int jm = s < 2 ? h + 16 * s : NMLP;
  const bool is_mlp = jm < NMLP;
  const __amdgpu_buffer_rsrc_t ws = __builtin_amdgcn_make_buffer_rsrc((void*)a.ws, 0, WS_BYTES, 0x00020000);

  const size_t wbase = ((size_t)h * SK + (size_t)s * (SK / NSPLIT) + (size_t)wave * KW) * (DH * 2);
  const char* Kw = a.Kc + wbase;
  const char* Vw = a.Vc + wbase;
  const int r0 = (b * 37 + wave * 5) & (2 * NGRP - 1);
  const unsigned loff = lane * 16;
  auto ld4 = [&](const char* base, int sub, v4u* dst) {
    const char* p = base + (size_t)((sub + r0) & (2 * NGRP - 1)) * 4096;
#pragma unroll
    for (int i = 0; i < 4; ++i) dst[i] = __builtin_nontemporal_load((const v4u*)(p + loff + i * 1024));
  };

  const v4u xraw = *(const v4u*)(a.x + min(lane, 55) * 8);
  u64 tag = 0;
  if (wave == 7) {
    const v2u e = ld64(ws, WS_EPOCH);
    tag = (((u64)e[1] << 32) | e[0]) * 6364136223846793005ull + 1442695040888963407ull;
    const v4u sent = {SENT, SENT, SENT, SENT};
    st128(ws, WS_PO + b * 1024 + lane * 16, sent);
    if (lane < 7) st128(ws, WS_Y + (h * 448 + s * 28) * 4 + lane * 16, sent);
    if (lane == 0) st64(ws, WS_PML + b * 8, v2u{SENT, SENT});
    if (is_mlp) { st128(ws, WS_OP + jm * 1792 + lane * 16, sent); if (lane < 48) st128(ws, WS_OP + jm * 1792 + 1024 + lane * 16, sent); }
  }
  v4u A[8], B[8];
  ld4(Kw, 0, A);
  constexpr int QI = 224 / WAVES;
  v4u wv[QI];
  {
    const u16* wp = a.wq + (size_t)(2 * QI * wave + hf) * (H * DH) + h * DH + c * 8;
#pragma unroll
    for (int i = 0; i < QI; ++i) wv[i] = *(const v4u*)(wp + (size_t)2 * i * (H * DH));
  }
  ld4(Kw, 1, A + 4);
  SCHED();
  float* xn = lds + L_XN + wave * 448;
  {
    float xv[8];
#pragma unroll
    for (int k = 0; k < 4; ++k) { xv[2 * k] = lo16(xraw[k]); xv[2 * k + 1] = hi16(xraw[k]); }
    const bool act = lane < 56;
    float sm = 0.f;
#pragma unroll
    for (int k = 0; k < 8; ++k) sm += xv[k];
    const float mean = wave_sum(act ? sm : 0.f) * (1.f / D);
    float v = 0.f;
#pragma unroll
    for (int k = 0; k < 8; ++k) { const float d = xv[k] - mean; v = fmaf(d, d, v); }
    const float var = bf16r(wave_sum(act ? v : 0.f) * (1.f / D));
    const float mb = bf16r(mean), sd = bf16r(sqrtf(bf16r(var + 1e-5f)));
    if (act) {
      *(v4f*)(xn + lane * 4) = v4f{ln_apply(xv[0], mb, sd), ln_apply(xv[2], mb, sd), ln_apply(xv[4], mb, sd), ln_apply(xv[6], mb, sd)};
      *(v4f*)(xn + 224 + lane * 4) = v4f{ln_apply(xv[1], mb, sd), ln_apply(xv[3], mb, sd), ln_apply(xv[5], mb, sd), ln_apply(xv[7], mb, sd)};
    }
  }
  float acc[8];
#pragma unroll
  for (int k = 0; k < 8; ++k) acc[k] = 0.f;
#pragma unroll
  for (int i4 = 0; i4 < QI / 4; ++i4) {
    const v4f xr = *(const v4f*)(xn + hf * 224 + QI * wave + i4 * 4);
#pragma unroll
    for (int i = 0; i < 4; ++i)
#pragma unroll
      for (int k = 0; k < 4; ++k) { acc[2 * k] = fmaf(xr[i], lo16(wv[i4 * 4 + i][k]), acc[2 * k]); acc[2 * k + 1] = fmaf(xr[i], hi16(wv[i4 * 4 + i][k]), acc[2 * k + 1]); }
  }
  float* red = lds + L_RED;
#pragma unroll
  for (int k = 0; k < 8; ++k) acc[k] = swap32_add(acc[k], acc[k]);
  if (hf == 0) {
    *(v4f*)(red + wave * 264 + c * 8) = v4f{acc[0], acc[1], acc[2], acc[3]};
    *(v4f*)(red + wave * 264 + c * 8 + 4) = v4f{acc[4], acc[5], acc[6], acc[7]};
  }
  __syncthreads();
  v4u q;
  {
    v4f qa = {0, 0, 0, 0}, qb = {0, 0, 0, 0};
#pragma unroll
    for (int w = 0; w < WAVES; ++w) { qa += *(const v4f*)(red + w * 264 + c * 8); qb += *(const v4f*)(red + w * 264 + c * 8 + 4); }
    q[0] = packbf(bf16r(qa[0]), bf16r(qa[1])); q[1] = packbf(bf16r(qa[2]), bf16r(qa[3]));
    q[2] = packbf(bf16r(qb[0]), bf16r(qb[1])); q[3] = packbf(bf16r(qb[2]), bf16r(qb[3]));
  }
  if (wave == 7) {
    DRAIN();
    if (lane == 0) st64(ws, WS_FLAG + b * 8, v2u{(unsigned)tag, (unsigned)(tag >> 32)});
  }

  float* scw = lds + L_SC + wave * KW;
  const bool b3 = lane & 8, b2 = lane & 4;
  const int vidx = ((lane >> 4) & 1) * 4 + ((lane >> 3) & 1) * 2 + ((lane >> 2) & 1);
  auto qk = [&](const v4u (&T)[8], int g) {
    float xx[8];
#pragma unroll
    for (int i = 0; i < 8; ++i) { float d = dot2(T[i][0], q[0], 0.f); d = dot2(T[i][1], q[1], d); d = dot2(T[i][2], q[2], d); xx[i] = dot2(T[i][3], q[3], d); }
    float y[4];
#pragma unroll
    for (int i = 0; i < 4; ++i) y[i] = swap16_add(xx[i], xx[i + 4]);
    float z[2];
#pragma unroll
    for (int i = 0; i < 2; ++i) { const float t = b3 ? y[i + 2] : y[i], u = b3 ? y[i] : y[i + 2]; z[i] = t + dpp<XOR8>(u); }
    const float t = b2 ? z[1] : z[0], u = b2 ? z[0] : z[1];
    float w = t + dpp<XOR7>(u);
    w += dpp<XOR2>(w); w += dpp<XOR1>(w);
    scw[hf * (KW / 2) + g * 8 + vidx] = bf16r(w) * 0.0625f;
  };
  auto pv = [&](const v4u (&T)[8], int g) {
    const v4f p0 = *(const v4f*)(scw + hf * (KW / 2) + g * 8), p1 = *(const v4f*)(scw + hf * (KW / 2) + g * 8 + 4);
#pragma unroll
    for (int i = 0; i < 8; ++i) {
      const float p = i < 4 ? p0[i & 3] : p1[i & 3];
#pragma unroll
      for (int k = 0; k < 4; ++k) { acc[2 * k] = fmaf(p, lo16(T[i][k]), acc[2 * k]); acc[2 * k + 1] = fmaf(p, hi16(T[i][k]), acc[2 * k + 1]); }
    }
  };
  for (int g = 0; g < NGRP; g += 2) {
    ld4(Kw, 2 * g + 2, B); ld4(Kw, 2 * g + 3, B + 4);
    SCHED();
    qk(A, g);
    SCHED();
    const bool more = g + 2 < NGRP;
    const char* nb = more ? Kw : Vw;
    const int ns = more ? 2 * g + 4 : 0;
    ld4(nb, ns, A); ld4(nb, ns + 1, A + 4);
    SCHED();
    qk(B, g + 1);
    SCHED();
  }
  float m, l;
  {
    v4f sv = *(const v4f*)(scw + lane * 4);
    m = wave_max(fmaxf(fmaxf(sv[0], sv[1]), fmaxf(sv[2], sv[3])));
#pragma unroll
    for (int k = 0; k < 4; ++k) sv[k] = __builtin_amdgcn_exp2f((sv[k] - m) * L2E);
    l = wave_sum((sv[0] + sv[1]) + (sv[2] + sv[3]));
    *(v4f*)(scw + lane * 4) = sv;
  }
  v2u woraw[4];
  {
    const int quad = lane & 7, cg = min(lane >> 3, 6);
    const u16* p = a.wo + (size_t)(h * 256 + 32 * wave + 4 * quad) * D + 28 * s + 4 * cg;
#pragma unroll
    for (int r = 0; r < 4; ++r) woraw[r] = *(const v2u*)(p + r * D);
  }
  v2u w1raw[14]; v4u w2raw[8]; v2u x4raw = {0, 0}, b2raw = {0, 0}; u16 b1raw = 0;
  if (is_mlp) {
    const u16* p1 = a.w1 + (size_t)(56 * wave + 14 * (lane >> 4)) * F + 64 * jm + 4 * (lane & 15);
#pragma unroll
    for (int r = 0; r < 14; ++r) w1raw[r] = *(const v2u*)(p1 + (size_t)r * F);
    const u16* p2 = a.w2 + (size_t)(64 * jm + 8 * wave) * D + 8 * min(lane, 55);
#pragma unroll
    for (int r = 0; r < 8; ++r) w2raw[r] = *(const v4u*)(p2 + r * D);
    x4raw = *(const v2u*)(a.x + 64 * min(wave, 6) + 4 * (lane & 15));
    b2raw = *(const v2u*)(a.b2 + 64 * min(jm, 6) + 4 * (lane & 15));
    b1raw = a.b1[64 * jm + 8 * wave + (lane & 7)];
  } else {
#pragma unroll
    for (int r = 0; r < 14; ++r) w1raw[r] = v2u{0, 0};
#pragma unroll
    for (int r = 0; r < 8; ++r) w2raw[r] = v4u{0, 0, 0, 0};
  }
  if (wave == 6) {
    const v2u e = ld64(ws, WS_EPOCH);
    const u64 tg = (((u64)e[1] << 32) | e[0]) * 6364136223846793005ull + 1442695040888963407ull;
    const unsigned tl = (unsigned)tg, th = (unsigned)(tg >> 32);
    for (unsigned spins = 0; spins < SPIN_CAP; ++spins) {
      CBAR();
      const v4u f0 = ld128(ws, WS_FLAG + lane * 32), f1 = ld128(ws, WS_FLAG + lane * 32 + 16);
      const bool ok = f0[0] == tl && f0[1] == th && f0[2] == tl && f0[3] == th && f1[0] == tl && f1[1] == th && f1[2] == tl && f1[3] == th;
      if (__all(ok)) break;
      __builtin_amdgcn_s_sleep(8);
    }
  }
#pragma unroll
  for (int k = 0; k < 8; ++k) acc[k] = 0.f;
  for (int g = 0; g < NGRP - 2; g += 2) {
    ld4(Vw, 2 * g + 2, B); ld4(Vw, 2 * g + 3, B + 4);
    SCHED();
    pv(A, g);
    SCHED();
    ld4(Vw, 2 * g + 4, A); ld4(Vw, 2 * g + 5, A + 4);
    SCHED();
    pv(B, g + 1);
    SCHED();
  }
  ld4(Vw, 2 * NGRP - 2, B); ld4(Vw, 2 * NGRP - 1, B + 4);
  SCHED();
  pv(A, NGRP - 2);
  unsigned wop[8], w1p[28], w2p[32];
#pragma unroll
  for (int t = 0; t < 2; ++t)
#pragma unroll
    for (int k = 0; k < 2; ++k) { wop[t * 4 + 2 * k] = perm_lo(woraw[2 * t][k], woraw[2 * t + 1][k]); wop[t * 4 + 2 * k + 1] = perm_hi(woraw[2 * t][k], woraw[2 * t + 1][k]); }
#pragma unroll
  for (int t = 0; t < 7; ++t)
#pragma unroll
    for (int k = 0; k < 2; ++k) { w1p[t * 4 + 2 * k] = perm_lo(w1raw[2 * t][k], w1raw[2 * t + 1][k]); w1p[t * 4 + 2 * k + 1] = perm_hi(w1raw[2 * t][k], w1raw[2 * t + 1][k]); }
#pragma unroll
  for (int t = 0; t < 4; ++t)
#pragma unroll
    for (int k = 0; k < 4; ++k) { w2p[t * 8 + 2 * k] = perm_lo(w2raw[2 * t][k], w2raw[2 * t + 1][k]); w2p[t * 8 + 2 * k + 1] = perm_hi(w2raw[2 * t][k], w2raw[2 * t + 1][k]); }
  SCHED();
  pv(B, NGRP - 1);
  red = lds + L_RED2;
#pragma unroll
  for (int k = 0; k < 8; ++k) acc[k] = swap32_add(acc[k], acc[k]);
  if (hf == 0) {
    *(v4f*)(red + wave * 264 + c * 8) = v4f{acc[0], acc[1], acc[2], acc[3]};
    *(v4f*)(red + wave * 264 + c * 8 + 4) = v4f{acc[4], acc[5], acc[6], acc[7]};
  }
  if (lane == 0) { red[wave * 264 + 256] = m; red[wave * 264 + 257] = l; }
  __syncthreads();
  if (wave == 0) {
    float mw[WAVES], M = -INFINITY;
#pragma unroll
    for (int w = 0; w < WAVES; ++w) { mw[w] = red[w * 264 + 256]; M = fmaxf(M, mw[w]); }
    v4f o = {0, 0, 0, 0}; float L = 0.f;
#pragma unroll
    for (int w = 0; w < WAVES; ++w) { const float f = __builtin_amdgcn_exp2f((mw[w] - M) * L2E); o += f * *(const v4f*)(red + w * 264 + lane * 4); L = fmaf(f, red[w * 264 + 257], L); }
    st128(ws, WS_PO + b * 1024 + lane * 16, nosent4(o));
    if (lane == 0) st64(ws, WS_PML + b * 8, v2u{nosent(M), nosent(L)});
  }
  {
    const int sub = lane >> 3, quad = lane & 7;
    const int offA = WS_PO + (h * 16 + sub) * 1024 + wave * 128 + quad * 16, offM = WS_PML + (h * 16 + sub) * 8;
    v4u pa, pb; v2u ma, mb2;
    for (unsigned spins = 0; spins < SPIN_CAP; ++spins) {
      CBAR();
      pa = ld128(ws, offA); pb = ld128(ws, offA + 8192); ma = ld64(ws, offM); mb2 = ld64(ws, offM + 64);
      const bool ok = full4(pa) && full4(pb) && ma[0] != SENT && ma[1] != SENT && mb2[0] != SENT && mb2[1] != SENT;
      if (__all(ok)) break;
      __builtin_amdgcn_s_sleep(4);
    }
    const float M = max_hi3(fmaxf(asf(ma[0]), asf(mb2[0])));
    const float fa = __builtin_amdgcn_exp2f((asf(ma[0]) - M) * L2E), fb = __builtin_amdgcn_exp2f((asf(mb2[0]) - M) * L2E);
    const float L = sum_hi3(fmaf(fa, asf(ma[1]), fb * asf(mb2[1])));
    const v4f o = fa * asf4(pa) + fb * asf4(pb);
    float ov[4];
#pragma unroll
    for (int k = 0; k < 4; ++k) ov[k] = bf16r(sum_hi3(o[k]) / L);
    const unsigned o01 = packbf(ov[0], ov[1]), o23 = packbf(ov[2], ov[3]);
    v4f y;
#pragma unroll
    for (int k = 0; k < 4; ++k) y[k] = sum_lo3(dot2(wop[4 + k], o23, dot2(wop[k], o01, 0.f)));
    if (quad == 0) *(v4f*)(lds + L_Y + wave * 32 + sub * 4) = y;
  }
  __syncthreads();
  if (wave == 1 && lane < 7) {
    v4f y = {0, 0, 0, 0};
#pragma unroll
    for (int w = 0; w < WAVES; ++w) y += *(const v4f*)(lds + L_Y + w * 32 + lane * 4);
    st128(ws, WS_Y + (h * 448 + s * 28) * 4 + lane * 16, nosent4(y));
  }
  if (!is_mlp) return;
  float* hb = lds + L_H;
  if (wave < 7) {
    const int off = WS_Y + ((lane >> 4) * 448 + wave * 64 + (lane & 15) * 4) * 4;
    v4u y0, y1, y2, y3;
    for (unsigned spins = 0; spins < SPIN_CAP; ++spins) {
      CBAR();
      y0 = ld128(ws, off); y1 = ld128(ws, off + 4 * 1792); y2 = ld128(ws, off + 8 * 1792); y3 = ld128(ws, off + 12 * 1792);
      if (__all(full4(y0) && full4(y1) && full4(y2) && full4(y3))) break;
      __builtin_amdgcn_s_sleep(4);
    }
    const v4f ys = (asf4(y0) + asf4(y1)) + (asf4(y2) + asf4(y3));
    v4f hv;
#pragma unroll
    for (int k = 0; k < 4; ++k) {
      float t = swap16_add(ys[k], ys[k]); t = swap32_add(t, t);
      const float xv = (k & 1) ? hi16(x4raw[k >> 1]) : lo16(x4raw[k >> 1]);
      hv[k] = bf16r(xv + bf16r(t));
    }
    if (lane < 16) *(v4f*)(hb + wave * 64 + lane * 4) = hv;
  }
  __syncthreads();
  {
    float hv[7];
#pragma unroll
    for (int j = 0; j < 7; ++j) hv[j] = hb[lane + 64 * j];
    float own[14];
#pragma unroll
    for (int t = 0; t < 7; ++t) { const v2f v = *(const v2f*)(hb + 56 * wave + 14 * (lane >> 4) + 2 * t); own[2 * t] = v[0]; own[2 * t + 1] = v[1]; }
    float mb, sd;
    ln_stats(hv, mb, sd);
    float y[4] = {0.f, 0.f, 0.f, 0.f};
#pragma unroll
    for (int t = 0; t < 7; ++t) {
      const unsigned xp = packbf(ln_apply(own[2 * t], mb, sd), ln_apply(own[2 * t + 1], mb, sd));
#pragma unroll
      for (int k = 0; k < 4; ++k) y[k] = dot2(w1p[t * 4 + k], xp, y[k]);
    }
    const float z0 = swap16_add(y[0], y[1]), z1 = swap16_add(y[2], y[3]);
    const float z = swap32_add(z0, z1);
    lds[L_H1 + wave * 64 + (lane & 15) * 4 + ((lane >> 4) & 1) + 2 * (lane >> 5)] = z;
  }
  __syncthreads();
  {
    float v = sum_hi3(lds[L_H1 + (lane >> 3) * 64 + 8 * wave + (lane & 7)]);
    v = bf16r(bf16r(v) + bf2f(b1raw));
    v = v > 0.f ? v : 0.f;
    v = bf16r(v * v);
    const unsigned pr = packbf(v, dpp<XOR1>(v));
    float y[8];
#pragma unroll
    for (int k = 0; k < 8; ++k) y[k] = 0.f;
#pragma unroll
    for (int t = 0; t < 4; ++t) {
      const unsigned hp = __builtin_amdgcn_readlane(pr, 2 * t);
#pragma unroll
      for (int k = 0; k < 8; ++k) y[k] = dot2(w2p[t * 8 + k], hp, y[k]);
    }
    if (lane < 56) {
      *(v4f*)(lds + L_OP + wave * 448 + lane * 8) = v4f{y[0], y[1], y[2], y[3]};
      *(v4f*)(lds + L_OP + wave * 448 + lane * 8 + 4) = v4f{y[4], y[5], y[6], y[7]};
    }
  }
  __syncthreads();
  if (threadIdx.x < 112) {
    v4f y = {0, 0, 0, 0};
#pragma unroll
    for (int w = 0; w < WAVES; ++w) y += *(const v4f*)(lds + L_OP + w * 448 + threadIdx.x * 4);
    st128(ws, WS_OP + jm * 1792 + threadIdx.x * 16, nosent4(y));
  }
  if (jm < NFIN && wave == 7) {
    const int off = WS_OP + (lane >> 4) * 1792 + jm * 256 + (lane & 15) * 16;
    v4u p[7];
    for (unsigned spins = 0; spins < SPIN_CAP; ++spins) {
      CBAR();
      bool ok = true;
#pragma unroll
      for (int i = 0; i < 7; ++i) { p[i] = ld128(ws, off + i * 4 * 1792); ok = ok && full4(p[i]); }
      if (__all(ok)) break;
      __builtin_amdgcn_s_sleep(2);
    }
    v4f ys = asf4(p[0]);
#pragma unroll
    for (int i = 1; i < 7; ++i) ys += asf4(p[i]);
    const v4f hv = *(const v4f*)(hb + jm * 64 + (lane & 15) * 4);
    float o[4];
#pragma unroll
    for (int k = 0; k < 4; ++k) {
      float t = swap16_add(ys[k], ys[k]); t = swap32_add(t, t);
      const float bv = (k & 1) ? hi16(b2raw[k >> 1]) : lo16(b2raw[k >> 1]);
      o[k] = bf16r(hv[k] + bf16r(bf16r(t) + bv));
    }
    if (lane < 16) *(v2u*)(a.out + jm * 64 + lane * 4) = v2u{packbf(o[0], o[1]), packbf(o[2], o[3])};
    if (jm == 0 && lane == 0) {
      const v2u e = ld64(ws, WS_EPOCH);
      const u64 tg = (((u64)e[1] << 32) | e[0]) * 6364136223846793005ull + 1442695040888963407ull;
      st64(ws, WS_EPOCH, v2u{(unsigned)tg, (unsigned)(tg >> 32)});
    }
  }
}
}

extern "C" void kernel_launch(void* const* d_in, const int* in_sizes, int n_in, void* d_out, int out_size, void* d_ws, size_t ws_size, hipStream_t stream) {
  Args a;
  a.x = (const u16*)d_in[0]; a.Kc = (const char*)d_in[1]; a.Vc = (const char*)d_in[2]; a.wq = (const u16*)d_in[3]; a.wo = (const u16*)d_in[4];
  a.w1 = (const u16*)d_in[6]; a.b1 = (const u16*)d_in[7]; a.w2 = (const u16*)d_in[8]; a.b2 = (const u16*)d_in[9];
  a.out = (u16*)d_out; a.ws = (char*)d_ws;
  (void)hipFuncSetAttribute((const void*)fused_kernel, hipFuncAttributeMaxDynamicSharedMemorySize, LDS_BYTES);
  fused_kernel<<<256, WAVES * 64, LDS_BYTES, stream>>>(a);
}
```

```cpp
#include <hip/hip_runtime.h>
#include <hip/hip_bf16.h>
#include <stdint.h>

namespace {
constexpr int D = 448, H = 16, SK = 32768, DH = 256, F = 1792;
constexpr int NL = 2, SW = 4, TW = 8, NTHR = (NL + SW + TW) * 64;
constexpr int NMLP = 28, NFIN = 7;

constexpr int WIN_AHEAD = 8, WIN_EARLY = 10, WIN_ON = 11, WIN_LATE = 12, WIN_FAR = 14, WIN_START = 14, WIN_MAX = 14;
constexpr int LAG_SHIFT = 5, FAR_STEPS = 3, START_TILES = 24;
constexpr int SCHED_OFF = 150;
constexpr int SCHED_TAU = 7200;

constexpr float L2E = 1.4426950408889634f;
constexpr unsigned EMPTY = 0xFFFFFFFFu;
constexpr unsigned SPIN_CAP = 1u << 16;
constexpr unsigned LDS_SPIN_CAP = 1u << 22;
constexpr int AUX_SC1 = 16;

constexpr int WS_EPOCH = 0;
constexpr int WS_FLAG = 256;
constexpr int WS_Y = 8192;
constexpr int WS_OP = WS_Y + 256 * 128;
constexpr int WS_PML = 114688;
constexpr int WS_QB = WS_PML + 256 * 128;
constexpr int WS_PO = 262144;
constexpr int WS_BYTES = WS_PO + 256 * 1024;
static_assert(WS_OP + NMLP * D * 4 <= WS_PML && WS_QB + H * 2048 <= WS_PO, "workspace map");

constexpr int L_RING = 0;
constexpr int L_SC = 16 * 8192;
constexpr int L_Q = L_SC + SW * 2048;
constexpr int L_RED = L_Q + 512;
constexpr int L_ML = L_RED + SW * 1024;
constexpr int L_FAC = L_ML + 32;
constexpr int L_CNT = L_FAC + 16;
constexpr int L_LAND = L_CNT + 16;
constexpr int L_FREE = L_LAND + 16;
constexpr int L_Y = L_FREE + 16;
constexpr int L_H = L_Y + TW * 32 * 4;
constexpr int L_H1 = L_H + D * 2;
constexpr int L_OP = L_H1 + TW * 64 * 4;
constexpr int LDS_BYTES = L_OP + TW * D * 4;
static_assert(LDS_BYTES <= 160 * 1024 && L_H % 16 == 0 && L_OP % 16 == 0, "LDS map");

typedef unsigned v4u __attribute__((ext_vector_type(4)));
typedef unsigned v2u __attribute__((ext_vector_type(2)));
typedef float v4f __attribute__((ext_vector_type(4)));
typedef float v2f __attribute__((ext_vector_type(2)));
typedef __bf16 bf16x2 __attribute__((ext_vector_type(2)));
typedef unsigned short u16;
typedef unsigned long long u64;
#define LAS __attribute__((address_space(3)))
typedef LAS char* lptr;
typedef __amdgpu_buffer_rsrc_t rsrc_t;

#define DRAIN() asm volatile("s_waitcnt vmcnt(0)" ::: "memory")
#define CBAR() asm volatile("" ::: "memory")
#define BAR0() do { __builtin_amdgcn_sched_barrier(0); asm volatile("s_waitcnt lgkmcnt(0)\n\ts_barrier" ::: "memory"); __builtin_amdgcn_sched_barrier(0); } while (0)
#define PACE() do { DRAIN(); __builtin_amdgcn_s_sleep(20); } while (0)

__device__ __forceinline__ float asf(unsigned v) { return __builtin_bit_cast(float, v); }
__device__ __forceinline__ unsigned asu(float v) { return __builtin_bit_cast(unsigned, v); }
__device__ __forceinline__ v4f asf4(v4u v) { return __builtin_bit_cast(v4f, v); }
__device__ __forceinline__ float bf16r(float x) { return (float)(__bf16)x; }
__device__ __forceinline__ float lo16(unsigned v) { return asf(v << 16); }
__device__ __forceinline__ float hi16(unsigned v) { return asf(v & 0xffff0000u); }
__device__ __forceinline__ unsigned pk_bf16(float lo, float hi) { const bf16x2 v = {(__bf16)lo, (__bf16)hi}; return __builtin_bit_cast(unsigned, v); }
__device__ __forceinline__ unsigned perm_lo(unsigned a, unsigned b) { return __builtin_amdgcn_perm(b, a, 0x05040100u); }
__device__ __forceinline__ unsigned perm_hi(unsigned a, unsigned b) { return __builtin_amdgcn_perm(b, a, 0x07060302u); }
__device__ __forceinline__ float dot2(unsigned a, unsigned b, float acc) {
  return __builtin_amdgcn_fdot2_f32_bf16(__builtin_bit_cast(bf16x2, a), __builtin_bit_cast(bf16x2, b), acc, false);
}

template <int CTRL> __device__ __forceinline__ float dpp(float x) {
  return asf(__builtin_amdgcn_mov_dpp(__builtin_bit_cast(int, x), CTRL, 0xf, 0xf, true));
}
constexpr int XOR1 = 0xB1, XOR2 = 0x4E, XOR7 = 0x141, XOR8 = 0x128;
__device__ __forceinline__ float swap16_add(float a, float b) {
  auto s = __builtin_amdgcn_permlane16_swap(asu(a), asu(b), false, false);
  return asf(s[0]) + asf(s[1]);
}
__device__ __forceinline__ float swap32_add(float a, float b) {
  auto s = __builtin_amdgcn_permlane32_swap(asu(a), asu(b), false, false);
  return asf(s[0]) + asf(s[1]);
}
__device__ __forceinline__ float sum_hi3(float v) { v += dpp<XOR8>(v); v = swap16_add(v, v); return swap32_add(v, v); }
__device__ __forceinline__ float sum_lo3(float v) { v += dpp<XOR1>(v); v += dpp<XOR2>(v); return v + dpp<XOR7>(v); }
__device__ __forceinline__ float wave_sum(float v) { return sum_hi3(sum_lo3(v)); }
__device__ __forceinline__ float wave_max(float v) {
  v = fmaxf(v, dpp<XOR1>(v)); v = fmaxf(v, dpp<XOR2>(v)); v = fmaxf(v, dpp<XOR7>(v)); v = fmaxf(v, dpp<XOR8>(v));
  auto s = __builtin_amdgcn_permlane16_swap(asu(v), asu(v), false, false);
  v = fmaxf(asf(s[0]), asf(s[1]));
  auto t = __builtin_amdgcn_permlane32_swap(asu(v), asu(v), false, false);
  return fmaxf(asf(t[0]), asf(t[1]));
}
__device__ __forceinline__ float reduce8(const float (&x)[8], int lane) {
  const bool b3 = lane & 8, b2 = lane & 4;
  float y[4];
#pragma unroll
  for (int i = 0; i < 4; ++i) y[i] = swap16_add(x[i], x[i + 4]);
  float z[2];
#pragma unroll
  for (int i = 0; i < 2; ++i) { const float t = b3 ? y[i + 2] : y[i], u = b3 ? y[i] : y[i + 2]; z[i] = t + dpp<XOR8>(u); }
  const float t = b2 ? z[1] : z[0], u = b2 ? z[0] : z[1];
  float w = t + dpp<XOR7>(u);
  w += dpp<XOR2>(w);
  return w + dpp<XOR1>(w);
}
__device__ __forceinline__ int reduce8_index(int lane) { return ((lane >> 4) & 1) * 4 + ((lane >> 3) & 1) * 2 + ((lane >> 2) & 1); }
__device__ __forceinline__ float reduce4(float a, float b, float c, float d) { return swap32_add(swap16_add(a, b), swap16_add(c, d)); }
__device__ __forceinline__ float ln_rstd(float var) { return __builtin_amdgcn_rcpf(bf16r(__builtin_amdgcn_sqrtf(bf16r(bf16r(var) + 1e-5f)))); }

__device__ __forceinline__ u64 next_tag(v2u e) { return (((u64)e[1] << 32) | e[0]) * 6364136223846793005ull + 1442695040888963407ull; }
__device__ __forceinline__ unsigned fill(float v) { return min(asu(v), EMPTY - 1); }
__device__ __forceinline__ v4u fill4(v4f v) { return v4u{fill(v[0]), fill(v[1]), fill(v[2]), fill(v[3])}; }
__device__ __forceinline__ bool missing(v4u v) {
  return __builtin_amdgcn_ballot_w64(max(max(max(v[0], v[1]), v[2]), v[3]) == EMPTY) != 0;
}
__device__ __forceinline__ v4u ld128(rsrc_t rs, int off) { return __builtin_amdgcn_raw_buffer_load_b128(rs, off, 0, AUX_SC1); }
__device__ __forceinline__ v2u ld64(rsrc_t rs, int off) { return __builtin_amdgcn_raw_buffer_load_b64(rs, off, 0, AUX_SC1); }
__device__ __forceinline__ unsigned ld32(rsrc_t rs, int off) { return __builtin_amdgcn_raw_buffer_load_b32(rs, off, 0, AUX_SC1); }
__device__ __forceinline__ void st128(rsrc_t rs, int off, v4u v) { __builtin_amdgcn_raw_buffer_store_b128(v, rs, off, 0, AUX_SC1); }
__device__ __forceinline__ void st64(rsrc_t rs, int off, v2u v) { __builtin_amdgcn_raw_buffer_store_b64(v, rs, off, 0, AUX_SC1); }
__device__ __forceinline__ void st32(rsrc_t rs, int off, unsigned v) { __builtin_amdgcn_raw_buffer_store_b32(v, rs, off, 0, AUX_SC1); }
constexpr int OOB = 0x7FFFFFF0;

template <typename T> __device__ __forceinline__ T lds_ld(lptr p) { return *(const LAS T*)p; }
template <typename T> __device__ __forceinline__ void lds_st(lptr p, T v) { *(LAS T*)p = v; }
__device__ __forceinline__ unsigned lds_poll(lptr p) { return *(const volatile LAS unsigned*)p; }
__device__ __forceinline__ void lds_post(lptr p, unsigned v) { *(volatile LAS unsigned*)p = v; }
template <typename T> __device__ __forceinline__ T ld_nt(const void* p) { return __builtin_nontemporal_load((const T*)p); }

template <int WIN> __device__ __forceinline__ void dma4(unsigned voff, const char* gp, unsigned ldsaddr) {
  asm volatile("s_mov_b32 m0, %2\n\ts_nop 0\n\t"
               "s_waitcnt vmcnt(%3)\n\tglobal_load_lds_dwordx4 %0, %1 nt\n\t"
               "s_waitcnt vmcnt(%3)\n\tglobal_load_lds_dwordx4 %0, %1 offset:1024 nt\n\t"
               "s_waitcnt vmcnt(%3)\n\tglobal_load_lds_dwordx4 %0, %1 offset:2048 nt\n\t"
               "s_waitcnt vmcnt(%3)\n\tglobal_load_lds_dwordx4 %0, %1 offset:3072 nt"
               :: "v"(voff), "s"(gp), "s"(ldsaddr), "i"(WIN - 1) : "memory");
}

struct Args {
  const u16* x; const char* Kc; const char* Vc; const u16* wq; const u16* wo;
  const u16* w1; const u16* b1; const u16* w2; const u16* b2; u16* out; char* ws;
};

__device__ __forceinline__ void loader_wave(const Args& a, lptr L, int lane, int l, int b, unsigned t0) {
  __builtin_amdgcn_s_setprio(3);
  const char* Kp = a.Kc + ((size_t)b << 20);
  const char* Vp = a.Vc + ((size_t)b << 20) - (size_t)128 * 8192;
  const unsigned voff = lane * 16;
  const unsigned ring = (unsigned)(size_t)(L + L_RING);
  const lptr land = L + L_LAND + l * 4;
  constexpr int NTL = 256 / NL, KLAG = (WIN_MAX - 1) >> 3;
  static_assert(((WIN_MAX - 1) & 7) >= 4, "the landed count is posted behind the second half of a tile");
  BAR0();
  unsigned freed = 0;
  unsigned now = (unsigned)__builtin_amdgcn_s_memrealtime();
  asm volatile("" : "+s"(now));
  for (int k = 1; k < NTL; ++k) {
    const int T = k * NL + l;
    if (T >= 16) {
      const unsigned need = (T - 16) >> 2;
      unsigned f = __builtin_amdgcn_readfirstlane(freed);
      for (unsigned spins = 0; f <= need && spins < LDS_SPIN_CAP; ++spins) f = __builtin_amdgcn_readfirstlane(lds_poll(L + L_FREE + (T & 3) * 4));
    }
    freed = lds_poll(L + L_FREE + ((T + NL) & 3) * 4);
    int step = (int)(now - (((T - NL) * SCHED_TAU) >> 8) - (t0 + SCHED_OFF)) >> LAG_SHIFT;
    asm volatile("" : "+s"(step));
    u64 clock;
    asm volatile("s_memrealtime %0" : "=s"(clock) :: "memory");
    const char* src = (T < 128 ? Kp : Vp) + (size_t)T * 8192;
    const unsigned dst = ring + (T & 15) * 8192;
#define TILE(WIN) { dma4<WIN>(voff, src, dst); dma4<WIN>(voff, src + 4096, dst + 4096); if (lane == 0) lds_post(land, k - KLAG); CBAR(); }
    if (T < START_TILES) TILE(WIN_START)
    else if (step < -1) TILE(WIN_AHEAD)
    else if (step < 0) TILE(WIN_EARLY)
    else if (step < 1) TILE(WIN_ON)
    else if (step < FAR_STEPS) TILE(WIN_LATE)
    else TILE(WIN_FAR)
#undef TILE
    asm volatile("s_waitcnt lgkmcnt(0)" : "+s"(clock) :: "memory");
    now = (unsigned)clock;
  }
#pragma unroll
  for (int k = NTL - 1 - KLAG; k < NTL; ++k) {
    asm volatile("s_waitcnt vmcnt(%0)" :: "i"((NTL - 1 - k) * 8) : "memory");
    if (lane == 0) lds_post(land, k + 1);
    CBAR();
  }
}

__device__ __forceinline__ void consumer_wave(const Args& a, lptr L, int lane, int cw, int b) {
  const int hf = lane >> 5, c = lane & 31;
  if (cw == 0) {
    if (lane < 4) { lds_st<unsigned>(L + L_FAC + lane * 4, EMPTY); lds_st<unsigned>(L + L_LAND + lane * 4, 0u); lds_st<unsigned>(L + L_FREE + lane * 4, 0u); }
    if (lane < 2) lds_st<unsigned>(L + L_CNT + lane * 4, 0u);
  }
  BAR0();
  const lptr rl = L + L_RING + lane * 16;
  const lptr land = L + L_LAND + (cw % NL) * 4, freep = L + L_FREE + cw * 4;
  auto fetch = [&](int n, v4u (&T)[8]) {
    const int t = cw + 4 * n;
    for (unsigned spins = 0; (int)lds_poll(land) <= t / NL && spins < LDS_SPIN_CAP; ++spins) __builtin_amdgcn_s_sleep(1);
    CBAR();
    const lptr p = rl + (t & 15) * 8192;
#pragma unroll
    for (int j = 0; j < 8; ++j) T[j] = lds_ld<v4u>(p + j * 1024);
    asm volatile("s_waitcnt lgkmcnt(0)" : "+v"(T[0]), "+v"(T[1]), "+v"(T[2]), "+v"(T[3]), "+v"(T[4]), "+v"(T[5]), "+v"(T[6]), "+v"(T[7]) :: "memory");
    if (lane == 0) lds_post(freep, n + 1);
    CBAR();
  };
  v4u A[8], B[8], C[8];
  fetch(0, A);
  fetch(1, B);
  fetch(2, C);
  for (unsigned spins = 0; lds_poll(L + L_LAND + 8) == 0 && spins < LDS_SPIN_CAP; ++spins) __builtin_amdgcn_s_sleep(1);
  CBAR();
  const v4u q = lds_ld<v4u>(L + L_Q + c * 16);
  const lptr scw = L + L_SC + cw * 2048;
  const int vidx = reduce8_index(lane);
  auto qk = [&](const v4u (&T)[8], int n) {
    float x[8];
#pragma unroll
    for (int j = 0; j < 8; ++j) { float d = dot2(T[j][0], q[0], 0.f); d = dot2(T[j][1], q[1], d); d = dot2(T[j][2], q[2], d); x[j] = dot2(T[j][3], q[3], d); }
    lds_st<float>(scw + (hf * 256 + n * 8 + vidx) * 4, bf16r(reduce8(x, lane)) * 0.0625f);
  };
  qk(A, 0);
  qk(B, 1);
  qk(C, 2);
  for (int n = 3; n < 32; ++n) { fetch(n, A); qk(A, n); }
  {
    v4f s0 = lds_ld<v4f>(scw + lane * 32), s1 = lds_ld<v4f>(scw + lane * 32 + 16);
    const float m = wave_max(fmaxf(fmaxf(fmaxf(s0[0], s0[1]), fmaxf(s0[2], s0[3])), fmaxf(fmaxf(s1[0], s1[1]), fmaxf(s1[2], s1[3]))));
#pragma unroll
    for (int k = 0; k < 4; ++k) { s0[k] = __builtin_amdgcn_exp2f((s0[k] - m) * L2E); s1[k] = __builtin_amdgcn_exp2f((s1[k] - m) * L2E); }
    const float l = wave_sum(((s0[0] + s0[1]) + (s0[2] + s0[3])) + ((s1[0] + s1[1]) + (s1[2] + s1[3])));
    lds_st<v4u>(scw + lane * 16, v4u{pk_bf16(s0[0], s0[1]), pk_bf16(s0[2], s0[3]), pk_bf16(s1[0], s1[1]), pk_bf16(s1[2], s1[3])});
    if (lane == 0) {
      lds_st<v2f>(L + L_ML + cw * 8, v2f{m, l});
      CBAR();
      __hip_atomic_fetch_add((LAS unsigned*)(L + L_CNT), 1u, __ATOMIC_RELAXED, __HIP_MEMORY_SCOPE_WORKGROUP);
      CBAR();
    }
  }
  float acc[8];
#pragma unroll
  for (int k = 0; k < 8; ++k) acc[k] = 0.f;
  for (int n = 0; n < 32; ++n) {
    const v4u pp = lds_ld<v4u>(scw + (hf * 128 + n * 4) * 4);
    fetch(32 + n, A);
#pragma unroll
    for (int i = 0; i < 4; ++i)
#pragma unroll
      for (int k = 0; k < 4; ++k) {
        acc[2 * k] = dot2(perm_lo(A[2 * i][k], A[2 * i + 1][k]), pp[i], acc[2 * k]);
        acc[2 * k + 1] = dot2(perm_hi(A[2 * i][k], A[2 * i + 1][k]), pp[i], acc[2 * k + 1]);
      }
  }
  float fac = asf(lds_poll(L + L_FAC + cw * 4));
  for (unsigned spins = 0; asu(fac) == EMPTY && spins < LDS_SPIN_CAP; ++spins) { __builtin_amdgcn_s_sleep(1); fac = asf(lds_poll(L + L_FAC + cw * 4)); }
  float o[4];
#pragma unroll
  for (int k = 0; k < 4; ++k) o[k] = swap32_add(acc[k], acc[k + 4]) * fac;
  lds_st<v4f>(L + L_RED + cw * 1024 + c * 32 + hf * 16, v4f{o[0], o[1], o[2], o[3]});
  CBAR();
  unsigned old = 0;
  if (lane == 0) old = __hip_atomic_fetch_add((LAS unsigned*)(L + L_CNT + 4), 1u, __ATOMIC_RELAXED, __HIP_MEMORY_SCOPE_WORKGROUP);
  CBAR();
  if (__builtin_amdgcn_readfirstlane(old) == SW - 1) {
    const v4f sum = (lds_ld<v4f>(L + L_RED + lane * 16) + lds_ld<v4f>(L + L_RED + 1024 + lane * 16)) + (lds_ld<v4f>(L + L_RED + 2048 + lane * 16) + lds_ld<v4f>(L + L_RED + 3072 + lane * 16));
    const rsrc_t ws = __builtin_amdgcn_make_buffer_rsrc((void*)a.ws, 0, WS_BYTES, 0x00020000);
    st128(ws, WS_PO + b * 1024 + lane * 16, fill4(sum));
  }
}

__global__ void __launch_bounds__(NTHR, 4) fused_kernel(Args a) {
  extern __shared__ __attribute__((aligned(16))) char smem[];
  const lptr L = (lptr)smem;
  const int lane = threadIdx.x & 63, wave = __builtin_amdgcn_readfirstlane(threadIdx.x >> 6);
  const int hf = lane >> 5, c = lane & 31;
  const int b = (blockIdx.x & 7) * 32 + (blockIdx.x >> 3);
  const int h = b >> 4, s = b & 15;
  const unsigned t0 = (unsigned)__builtin_amdgcn_s_memrealtime();
  {
    const char* src = a.Kc + ((size_t)b << 20) + (size_t)wave * 8192;
    const unsigned dst = (unsigned)(size_t)(L + L_RING) + wave * 8192;
    asm volatile("s_cmp_lt_u32 %5, %6\n\ts_cbranch_scc0 .Lfirst%=\n\t"
                 "s_mov_b32 m0, %2\n\ts_nop 0\n\t"
                 "global_load_lds_dwordx4 %0, %1 nt\n\tglobal_load_lds_dwordx4 %0, %1 offset:1024 nt\n\t"
                 "global_load_lds_dwordx4 %0, %1 offset:2048 nt\n\tglobal_load_lds_dwordx4 %0, %1 offset:3072 nt\n\t"
                 "s_mov_b32 m0, %4\n\ts_nop 0\n\t"
                 "global_load_lds_dwordx4 %0, %3 nt\n\tglobal_load_lds_dwordx4 %0, %3 offset:1024 nt\n\t"
                 "global_load_lds_dwordx4 %0, %3 offset:2048 nt\n\tglobal_load_lds_dwordx4 %0, %3 offset:3072 nt\n"
                 ".Lfirst%=:"
                 :: "v"(lane * 16), "s"(src), "s"(dst), "s"(src + 4096), "s"(dst + 4096), "s"(wave), "i"(NL) : "memory", "scc");
  }
  if (wave < NL) { loader_wave(a, L, lane, wave, b, t0); return; }
  if (wave < NL + SW) { consumer_wave(a, L, lane, wave - NL, b); return; }

  const int tw = wave - NL - SW;
  const int jm = s < 2 ? h + 16 * s : NMLP;
  const bool is_mlp = jm < NMLP;
  const rsrc_t ws = __builtin_amdgcn_make_buffer_rsrc((void*)a.ws, 0, WS_BYTES, 0x00020000);
  if (tw != 0) BAR0();
  if (tw == 0) {
    const v4u xraw = *(const v4u*)(a.x + min(lane, 55) * 8);
    v4u wv[14];
    const u16* wp = a.wq + (size_t)c * (H * DH) + h * DH + 16 * s + 8 * hf;
#pragma unroll
    for (int i = 0; i < 14; ++i) wv[i] = ld_nt<v4u>(wp + (size_t)32 * i * (H * DH));
    BAR0();
    const v2u e = ld64(ws, WS_EPOCH);
    float xv[8];
#pragma unroll
    for (int k = 0; k < 4; ++k) { xv[2 * k] = lo16(xraw[k]); xv[2 * k + 1] = hi16(xraw[k]); }
    const bool act = lane < 56;
    float sm = 0.f;
#pragma unroll
    for (int k = 0; k < 8; ++k) sm += xv[k];
    const float mean = wave_sum(act ? sm : 0.f) * (1.f / D);
    float v = 0.f;
#pragma unroll
    for (int k = 0; k < 8; ++k) { const float d = xv[k] - mean; v = fmaf(d, d, v); }
    const float mb = bf16r(mean), rs = ln_rstd(wave_sum(act ? v : 0.f) * (1.f / D));
    if (act) {
      lds_st<v2u>(L + L_H + lane * 16, v2u{pk_bf16(bf16r(xv[0] - mb) * rs, bf16r(xv[1] - mb) * rs), pk_bf16(bf16r(xv[2] - mb) * rs, bf16r(xv[3] - mb) * rs)});
      lds_st<v2u>(L + L_H + lane * 16 + 8, v2u{pk_bf16(bf16r(xv[4] - mb) * rs, bf16r(xv[5] - mb) * rs), pk_bf16(bf16r(xv[6] - mb) * rs, bf16r(xv[7] - mb) * rs)});
    }
    float qa[8];
#pragma unroll
    for (int k = 0; k < 8; ++k) qa[k] = 0.f;
#pragma unroll
    for (int t = 0; t < 7; ++t) {
      const unsigned xp = (unsigned)lds_ld<u16>(L + L_H + (c + 64 * t) * 2) | ((unsigned)lds_ld<u16>(L + L_H + (c + 64 * t + 32) * 2) << 16);
#pragma unroll
      for (int k = 0; k < 4; ++k) {
        qa[2 * k] = dot2(perm_lo(wv[2 * t][k], wv[2 * t + 1][k]), xp, qa[2 * k]);
        qa[2 * k + 1] = dot2(perm_hi(wv[2 * t][k], wv[2 * t + 1][k]), xp, qa[2 * k + 1]);
      }
    }
    const float qv = bf16r(reduce8(qa, lane));
    const u64 tag = next_tag(e);
    const unsigned tlo = (unsigned)(tag >> 16) << 16, thi = (unsigned)(tag >> 32);
    if ((lane & 3) == 0) st64(ws, WS_QB + h * 2048 + (16 * s + 8 * hf + reduce8_index(lane)) * 8, v2u{tlo | (asu(qv) >> 16), thi});
    v4u g0 = {0, 0, 0, 0}, g1 = {0, 0, 0, 0};
    bool have = false;
    for (unsigned spins = 0; spins < SPIN_CAP; ++spins) {
      CBAR();
      const int off = have ? OOB : WS_QB + h * 2048 + lane * 32;
      const v4u n0 = ld128(ws, off), n1 = ld128(ws, off + 16);
      if (!have) { g0 = n0; g1 = n1; }
      have = (g0[0] & 0xffff0000u) == tlo && g0[1] == thi && (g0[2] & 0xffff0000u) == tlo && g0[3] == thi &&
             (g1[0] & 0xffff0000u) == tlo && g1[1] == thi && (g1[2] & 0xffff0000u) == tlo && g1[3] == thi;
      if (__all(have)) break;
      __builtin_amdgcn_s_sleep(4);
    }
    lds_st<v2u>(L + L_Q + lane * 8, v2u{perm_lo(g0[0], g0[2]), perm_lo(g1[0], g1[2])});
    CBAR();
    if (lane == 0) lds_post(L + L_LAND + 8, 1u);
  }
  if (tw == 1) {
    const v2u e = ld64(ws, WS_EPOCH);
    const v4u none = {EMPTY, EMPTY, EMPTY, EMPTY};
    st128(ws, WS_PO + b * 1024 + lane * 16, none);
    if (lane < 8) st128(ws, WS_Y + b * 128 + lane * 16, none);
    if (lane >= 8 && lane < 16) st128(ws, WS_PML + b * 128 + (lane - 8) * 16, none);
    if (is_mlp) { st128(ws, WS_OP + jm * 1792 + lane * 16, none); if (lane < 48) st128(ws, WS_OP + jm * 1792 + 1024 + lane * 16, none); }
    DRAIN();
    const u64 tag = next_tag(e);
    if (lane == 0) st64(ws, WS_FLAG + b * 8, v2u{(unsigned)tag, (unsigned)(tag >> 32)});
  }
  __builtin_amdgcn_s_sleep(127);
  __builtin_amdgcn_s_sleep(127);
  if (tw == 3) {
    const u64 tg = next_tag(ld64(ws, WS_EPOCH));
    const unsigned tl = (unsigned)tg, th = (unsigned)(tg >> 32);
    const bool mine = is_mlp || (lane >> 2) == h;
    for (unsigned spins = 0; spins < SPIN_CAP; ++spins) {
      CBAR();
      const v4u f0 = ld128(ws, mine ? WS_FLAG + lane * 32 : OOB), f1 = ld128(ws, mine ? WS_FLAG + lane * 32 + 16 : OOB);
      const bool ok = f0[0] == tl && f0[1] == th && f0[2] == tl && f0[3] == th && f1[0] == tl && f1[1] == th && f1[2] == tl && f1[3] == th;
      if (__all(ok || !mine)) break;
      __builtin_amdgcn_s_sleep(64);
    }
  }
  v2u woraw[4];
  {
    const u16* p = a.wo + (size_t)(h * 256 + 32 * tw + 4 * (lane & 7)) * D + 28 * s + 4 * min(lane >> 3, 6);
#pragma unroll
    for (int r = 0; r < 4; ++r) { woraw[r] = ld_nt<v2u>(p + r * D); PACE(); }
  }
  v2u w1raw[14]; v4u w2raw[8]; unsigned xraw1 = 0, b2raw = 0, b1raw = 0;
  const int col4 = (lane & 15) * 4 + ((lane >> 4) & 1) + 2 * (lane >> 5);
  if (is_mlp) {
    const u16* p1 = a.w1 + (size_t)(56 * tw + 14 * (lane >> 4)) * F + 64 * jm + 4 * (lane & 15);
#pragma unroll
    for (int r = 0; r < 14; ++r) { w1raw[r] = ld_nt<v2u>(p1 + (size_t)r * F); PACE(); }
    const u16* p2 = a.w2 + (size_t)(64 * jm + 8 * tw) * D + 8 * min(lane, 55);
#pragma unroll
    for (int r = 0; r < 8; ++r) { w2raw[r] = ld_nt<v4u>(p2 + r * D); PACE(); }
    xraw1 = a.x[64 * min(tw, 6) + col4];
    b2raw = a.b2[64 * min(jm, 6) + col4];
    b1raw = a.b1[64 * jm + 8 * tw + (lane & 7)];
  } else {
#pragma unroll
    for (int r = 0; r < 14; ++r) w1raw[r] = v2u{0, 0};
#pragma unroll
    for (int r = 0; r < 8; ++r) w2raw[r] = v4u{0, 0, 0, 0};
  }
  unsigned wop[8], w1p[28], w2p[32];
#pragma unroll
  for (int t = 0; t < 2; ++t)
#pragma unroll
    for (int k = 0; k < 2; ++k) { wop[t * 4 + 2 * k] = perm_lo(woraw[2 * t][k], woraw[2 * t + 1][k]); wop[t * 4 + 2 * k + 1] = perm_hi(woraw[2 * t][k], woraw[2 * t + 1][k]); }
#pragma unroll
  for (int t = 0; t < 7; ++t)
#pragma unroll
    for (int k = 0; k < 2; ++k) { w1p[t * 4 + 2 * k] = perm_lo(w1raw[2 * t][k], w1raw[2 * t + 1][k]); w1p[t * 4 + 2 * k + 1] = perm_hi(w1raw[2 * t][k], w1raw[2 * t + 1][k]); }
#pragma unroll
  for (int t = 0; t < 4; ++t)
#pragma unroll
    for (int k = 0; k < 4; ++k) { w2p[t * 8 + 2 * k] = perm_lo(w2raw[2 * t][k], w2raw[2 * t + 1][k]); w2p[t * 8 + 2 * k + 1] = perm_hi(w2raw[2 * t][k], w2raw[2 * t + 1][k]); }
  float csum;
  {
    float y[4] = {0.f, 0.f, 0.f, 0.f};
#pragma unroll
    for (int t = 0; t < 7; ++t)
#pragma unroll
      for (int k = 0; k < 4; ++k) y[k] = dot2(w1p[t * 4 + k], 0x3F803F80u, y[k]);
    csum = reduce4(y[0], y[1], y[2], y[3]);
  }
  const float xres = asf(xraw1 << 16), b1v = asf(b1raw << 16), b2v = asf(b2raw << 16);
  asm volatile("" : "+v"(csum));
#pragma unroll
  for (int i = 0; i < 8; ++i) asm volatile("" : "+v"(wop[i]));
#pragma unroll
  for (int i = 0; i < 28; ++i) asm volatile("" : "+v"(w1p[i]));
#pragma unroll
  for (int i = 0; i < 32; ++i) asm volatile("" : "+v"(w2p[i]));
  u64 tagf = 0;
  if (jm == 0 && tw == 7) { tagf = next_tag(ld64(ws, WS_EPOCH)); asm volatile("" : "+v"(tagf)); }

  if (tw == 3) {
    for (unsigned spins = 0; lds_poll(L + L_CNT) < SW && spins < LDS_SPIN_CAP; ++spins) __builtin_amdgcn_s_sleep(8);
    CBAR();
    const v2f ml = lds_ld<v2f>(L + L_ML + (lane & 3) * 8);
    float Mw = fmaxf(ml[0], dpp<XOR1>(ml[0])); Mw = fmaxf(Mw, dpp<XOR2>(Mw));
    float Lw = __builtin_amdgcn_exp2f((ml[0] - Mw) * L2E) * ml[1]; Lw += dpp<XOR1>(Lw); Lw += dpp<XOR2>(Lw);
    if (lane < 8) st128(ws, WS_PML + b * 128 + lane * 16, v4u{fill(Mw), fill(Lw), 0u, 0u});
    v2u pm;
    for (unsigned spins = 0; spins < SPIN_CAP; ++spins) {
      __builtin_amdgcn_s_sleep(100);
      CBAR();
      pm = ld64(ws, lane < 16 ? WS_PML + (h * 16 + lane) * 128 : OOB);
      if (__all(pm[0] != EMPTY && pm[1] != EMPTY)) break;
    }
    float M = fmaxf(asf(pm[0]), dpp<XOR1>(asf(pm[0]))); M = fmaxf(M, dpp<XOR2>(M)); M = fmaxf(M, dpp<XOR7>(M)); M = fmaxf(M, dpp<XOR8>(M));
    float Ls = __builtin_amdgcn_exp2f((asf(pm[0]) - M) * L2E) * asf(pm[1]); Ls += dpp<XOR1>(Ls); Ls += dpp<XOR2>(Ls); Ls += dpp<XOR7>(Ls); Ls += dpp<XOR8>(Ls);
    if (lane < 4) lds_st<unsigned>(L + L_FAC + lane * 4, fill(__builtin_amdgcn_exp2f((ml[0] - M) * L2E) / Ls));
    for (unsigned spins = 0; (int)lds_poll(L + L_LAND) < 256 / NL - 2 && spins < LDS_SPIN_CAP; ++spins) __builtin_amdgcn_s_sleep(16);
    for (unsigned spins = 0; spins < (SPIN_CAP << 2); ++spins) {
      CBAR();
      const unsigned ps = ld32(ws, lane < 16 ? WS_PO + (h * 16 + lane) * 1024 : OOB);
      if (__builtin_popcountll(__ballot(ps == EMPTY && lane != s)) <= 2) break;
      __builtin_amdgcn_s_sleep(4);
    }
  }
  __syncthreads();
  {
    const int sub = lane >> 3, quad = lane & 7;
    const int off = WS_PO + (h * 16 + sub) * 1024 + tw * 128 + quad * 16;
    v4u pa = ld128(ws, off), pb = ld128(ws, off + 8192);
    for (unsigned spins = 0; spins < SPIN_CAP; ++spins) {
      const bool ma = missing(pa), mb = missing(pb);
      if (!(ma || mb)) break;
      if (spins >= 2) __builtin_amdgcn_s_sleep(4);
      CBAR();
      if (ma) pa = ld128(ws, off);
      if (mb) pb = ld128(ws, off + 8192);
    }
    const v4f o = asf4(pa) + asf4(pb);
    const unsigned o01 = pk_bf16(sum_hi3(o[0]), sum_hi3(o[1])), o23 = pk_bf16(sum_hi3(o[2]), sum_hi3(o[3]));
    v4f y;
#pragma unroll
    for (int k = 0; k < 4; ++k) y[k] = sum_lo3(dot2(wop[4 + k], o23, dot2(wop[k], o01, 0.f)));
    if (quad == 0) lds_st<v4f>(L + L_Y + tw * 128 + sub * 16, y);
  }
  __syncthreads();
  if (tw == 1) {
    const lptr p = L + L_Y + hf * 512 + c * 4;
    float y = (lds_ld<float>(p) + lds_ld<float>(p + 128)) + (lds_ld<float>(p + 256) + lds_ld<float>(p + 384));
    y = swap32_add(y, y);
    if (lane < 32) st32(ws, WS_Y + b * 128 + lane * 4, lane < 28 ? fill(y) : 0u);
  }
  if (!is_mlp) return;
  if (tw < 7) {
    const int qd = tw * 16 + (lane & 15);
    const int off = WS_Y + (lane >> 4) * 2048 + (qd / 7) * 128 + (qd % 7) * 16;
    v4u y0 = ld128(ws, off), y1 = ld128(ws, off + 4 * 2048), y2 = ld128(ws, off + 8 * 2048), y3 = ld128(ws, off + 12 * 2048);
    for (unsigned spins = 0; spins < SPIN_CAP; ++spins) {
      const bool m0 = missing(y0), m1 = missing(y1), m2 = missing(y2), m3 = missing(y3);
      if (!(m0 || m1 || m2 || m3)) break;
      if (spins >= 2) __builtin_amdgcn_s_sleep(4);
      CBAR();
      if (m0) y0 = ld128(ws, off);
      if (m1) y1 = ld128(ws, off + 4 * 2048);
      if (m2) y2 = ld128(ws, off + 8 * 2048);
      if (m3) y3 = ld128(ws, off + 12 * 2048);
    }
    const v4f ys = (asf4(y0) + asf4(y1)) + (asf4(y2) + asf4(y3));
    const float at = reduce4(ys[0], ys[1], ys[2], ys[3]);
    lds_st<u16>(L + L_H + (tw * 64 + col4) * 2, (u16)(pk_bf16(xres + bf16r(at), 0.f) & 0xffffu));
  }
  __syncthreads();
  float rs;
  {
    const v4u hq = lds_ld<v4u>(L + L_H + min(lane, 55) * 16);
    unsigned hp[7];
#pragma unroll
    for (int t = 0; t < 7; ++t) hp[t] = lds_ld<unsigned>(L + L_H + (56 * tw + 14 * (lane >> 4) + 2 * t) * 2);
    const float c0 = lo16(lds_ld<unsigned>(L + L_H));
    float s1 = 0.f, s2 = 0.f;
#pragma unroll
    for (int k = 0; k < 4; ++k) { const float d0 = lo16(hq[k]) - c0, d1 = hi16(hq[k]) - c0; s1 += d0 + d1; s2 = fmaf(d0, d0, fmaf(d1, d1, s2)); }
    if (lane >= 56) { s1 = 0.f; s2 = 0.f; }
    float y[4] = {0.f, 0.f, 0.f, 0.f};
#pragma unroll
    for (int t = 0; t < 7; ++t)
#pragma unroll
      for (int k = 0; k < 4; ++k) y[k] = dot2(w1p[t * 4 + k], hp[t], y[k]);
    s1 = wave_sum(s1) * (1.f / D); s2 = wave_sum(s2) * (1.f / D);
    const float mb = bf16r(c0 + s1);
    rs = ln_rstd(fmaxf(fmaf(-s1, s1, s2), 0.f));
    lds_st<float>(L + L_H1 + (tw * 64 + col4) * 4, fmaf(-mb, csum, reduce4(y[0], y[1], y[2], y[3])));
  }
  __syncthreads();
  {
    float v = sum_hi3(lds_ld<float>(L + L_H1 + ((lane >> 3) * 64 + 8 * tw + (lane & 7)) * 4)) * rs;
    v = fmaxf(bf16r(bf16r(v) + b1v), 0.f);
    v = v * v;
    const unsigned pr = pk_bf16(v, dpp<XOR1>(v));
    float y[8];
#pragma unroll
    for (int k = 0; k < 8; ++k) y[k] = 0.f;
#pragma unroll
    for (int t = 0; t < 4; ++t) {
      const unsigned hp = __builtin_amdgcn_readlane(pr, 2 * t);
#pragma unroll
      for (int k = 0; k < 8; ++k) y[k] = dot2(w2p[t * 8 + k], hp, y[k]);
    }
    if (lane < 56) {
      lds_st<v4f>(L + L_OP + tw * 1792 + lane * 32, v4f{y[0], y[1], y[2], y[3]});
      lds_st<v4f>(L + L_OP + tw * 1792 + lane * 32 + 16, v4f{y[4], y[5], y[6], y[7]});
    }
  }
  __syncthreads();
  if (tw < 7) {
    const lptr p = L + L_OP + (tw * 64 + lane) * 4;
    const float y = ((lds_ld<float>(p) + lds_ld<float>(p + 1792)) + (lds_ld<float>(p + 2 * 1792) + lds_ld<float>(p + 3 * 1792))) +
                    ((lds_ld<float>(p + 4 * 1792) + lds_ld<float>(p + 5 * 1792)) + (lds_ld<float>(p + 6 * 1792) + lds_ld<float>(p + 7 * 1792)));
    st32(ws, WS_OP + jm * 1792 + (tw * 64 + lane) * 4, fill(y));
  }
  if (jm < NFIN && tw == 7) {
    const int off = WS_OP + (lane >> 4) * 1792 + jm * 256 + (lane & 15) * 16;
    const float hres = asf((unsigned)lds_ld<u16>(L + L_H + (jm * 64 + col4) * 2) << 16);
    v4u p[7];
#pragma unroll
    for (int i = 0; i < 7; ++i) p[i] = ld128(ws, off + i * 4 * 1792);
    for (unsigned spins = 0; spins < SPIN_CAP; ++spins) {
      bool m[7], any = false;
#pragma unroll
      for (int i = 0; i < 7; ++i) { m[i] = missing(p[i]); any = any || m[i]; }
      if (!any) break;
      CBAR();
#pragma unroll
      for (int i = 0; i < 7; ++i) if (m[i]) p[i] = ld128(ws, off + i * 4 * 1792);
    }
    const v4f ys = ((asf4(p[0]) + asf4(p[1])) + (asf4(p[2]) + asf4(p[3]))) + ((asf4(p[4]) + asf4(p[5])) + asf4(p[6]));
    const float t = reduce4(ys[0], ys[1], ys[2], ys[3]);
    const float o = bf16r(hres + bf16r(bf16r(t) + b2v));
    a.out[jm * 64 + col4] = (u16)(asu(o) >> 16);
    if (jm == 0 && lane == 0) st64(ws, WS_EPOCH, v2u{(unsigned)tagf, (unsigned)(tagf >> 32)});
  }
}
}

extern "C" void kernel_launch(void* const* d_in, const int* in_sizes, int n_in, void* d_out, int out_size, void* d_ws, size_t ws_size, hipStream_t stream) {
  Args a;
  a.x = (const u16*)d_in[0]; a.Kc = (const char*)d_in[1]; a.Vc = (const char*)d_in[2]; a.wq = (const u16*)d_in[3]; a.wo = (const u16*)d_in[4];
  a.w1 = (const u16*)d_in[6]; a.b1 = (const u16*)d_in[7]; a.w2 = (const u16*)d_in[8]; a.b2 = (const u16*)d_in[9];
  a.out = (u16*)d_out; a.ws = (char*)d_ws;
  (void)hipFuncSetAttribute((const void*)fused_kernel, hipFuncAttributeMaxDynamicSharedMemorySize, LDS_BYTES);
  fused_kernel<<<256, NTHR, LDS_BYTES, stream>>>(a);
}
```

```cpp
#include <hip/hip_runtime.h>
#include <hip/hip_bf16.h>
#include <stdint.h>

namespace {
constexpr int D = 448, H = 16, SK = 32768, DH = 256, F = 1792;
constexpr int NL = 2, SW = 4, TW = 8, NTHR = (NL + SW + TW) * 64;
constexpr int NMLP = 28, NFIN = 7;

constexpr int WIN_AHEAD = 8, WIN_EARLY = 10, WIN_ON = 11, WIN_LATE = 12, WIN_FAR = 14, WIN_START = 14, WIN_MAX = 14;
constexpr int LAG_SHIFT = 5, FAR_STEPS = 3, START_TILES = 24;
constexpr int SCHED_OFF = 150;
constexpr int TAU_K = 7300, TAU_V = 7100;

constexpr float L2E = 1.4426950408889634f;
constexpr unsigned EMPTY = 0xFFFFFFFFu;
constexpr unsigned SPIN_CAP = 1u << 16;
constexpr unsigned LDS_SPIN_CAP = 1u << 22;
constexpr int AUX_SC1 = 16;

constexpr int WS_EPOCH = 0;
constexpr int WS_FLAG = 256;
constexpr int WS_Y = 8192;
constexpr int WS_OP = WS_Y + 256 * 128;
constexpr int WS_PML = 114688;
constexpr int WS_QB = WS_PML + 256 * 128;
constexpr int WS_PO = 262144;
constexpr int WS_BYTES = WS_PO + 256 * 1024;
static_assert(WS_OP + NMLP * D * 4 <= WS_PML && WS_QB + H * 2048 <= WS_PO, "workspace map");

constexpr int L_RING = 0;
constexpr int L_SC = 16 * 8192;
constexpr int L_Q = L_SC + SW * 2048;
constexpr int L_RED = L_Q + 512;
constexpr int L_ML = L_RED + SW * 1024;
constexpr int L_FAC = L_ML + 32;
constexpr int L_CNT = L_FAC + 16;
constexpr int L_LAND = L_CNT + 16;
constexpr int L_FREE = L_LAND + 16;
constexpr int L_Y = L_FREE + 16;
constexpr int L_H = L_Y + TW * 32 * 4;
constexpr int L_H1 = L_H + D * 2;
constexpr int L_OP = L_H1 + TW * 64 * 4;
constexpr int LDS_BYTES = L_OP + TW * D * 4;
static_assert(LDS_BYTES <= 160 * 1024 && L_H % 16 == 0 && L_OP % 16 == 0, "LDS map");

typedef unsigned v4u __attribute__((ext_vector_type(4)));
typedef unsigned v2u __attribute__((ext_vector_type(2)));
typedef float v4f __attribute__((ext_vector_type(4)));
typedef float v2f __attribute__((ext_vector_type(2)));
typedef __bf16 bf16x2 __attribute__((ext_vector_type(2)));
typedef unsigned short u16;
typedef unsigned long long u64;
#define LAS __attribute__((address_space(3)))
typedef LAS char* lptr;
typedef __amdgpu_buffer_rsrc_t rsrc_t;

#define DRAIN() asm volatile("s_waitcnt vmcnt(0)" ::: "memory")
#define CBAR() asm volatile("" ::: "memory")
#define BAR0() do { __builtin_amdgcn_sched_barrier(0); asm volatile("s_waitcnt lgkmcnt(0)\n\ts_barrier" ::: "memory"); __builtin_amdgcn_sched_barrier(0); } while (0)
#define PACE() do { DRAIN(); __builtin_amdgcn_s_sleep(20); } while (0)

__device__ __forceinline__ float asf(unsigned v) { return __builtin_bit_cast(float, v); }
__device__ __forceinline__ unsigned asu(float v) { return __builtin_bit_cast(unsigned, v); }
__device__ __forceinline__ v4f asf4(v4u v) { return __builtin_bit_cast(v4f, v); }
__device__ __forceinline__ float bf16r(float x) { return (float)(__bf16)x; }
__device__ __forceinline__ float lo16(unsigned v) { return asf(v << 16); }
__device__ __forceinline__ float hi16(unsigned v) { return asf(v & 0xffff0000u); }
__device__ __forceinline__ unsigned pk_bf16(float lo, float hi) { const bf16x2 v = {(__bf16)lo, (__bf16)hi}; return __builtin_bit_cast(unsigned, v); }
__device__ __forceinline__ unsigned perm_lo(unsigned a, unsigned b) { return __builtin_amdgcn_perm(b, a, 0x05040100u); }
__device__ __forceinline__ unsigned perm_hi(unsigned a, unsigned b) { return __builtin_amdgcn_perm(b, a, 0x07060302u); }
__device__ __forceinline__ float dot2(unsigned a, unsigned b, float acc) {
  return __builtin_amdgcn_fdot2_f32_bf16(__builtin_bit_cast(bf16x2, a), __builtin_bit_cast(bf16x2, b), acc, false);
}

template <int CTRL> __device__ __forceinline__ float dpp(float x) {
  return asf(__builtin_amdgcn_mov_dpp(__builtin_bit_cast(int, x), CTRL, 0xf, 0xf, true));
}
constexpr int XOR1 = 0xB1, XOR2 = 0x4E, XOR7 = 0x141, XOR8 = 0x128;
__device__ __forceinline__ float swap16_add(float a, float b) {
  auto s = __builtin_amdgcn_permlane16_swap(asu(a), asu(b), false, false);
  return asf(s[0]) + asf(s[1]);
}
__device__ __forceinline__ float swap32_add(float a, float b) {
  auto s = __builtin_amdgcn_permlane32_swap(asu(a), asu(b), false, false);
  return asf(s[0]) + asf(s[1]);
}
__device__ __forceinline__ float sum_hi3(float v) { v += dpp<XOR8>(v); v = swap16_add(v, v); return swap32_add(v, v); }
__device__ __forceinline__ float sum_lo3(float v) { v += dpp<XOR1>(v); v += dpp<XOR2>(v); return v + dpp<XOR7>(v); }
__device__ __forceinline__ float wave_sum(float v) { return sum_hi3(sum_lo3(v)); }
__device__ __forceinline__ float wave_max(float v) {
  v = fmaxf(v, dpp<XOR1>(v)); v = fmaxf(v, dpp<XOR2>(v)); v = fmaxf(v, dpp<XOR7>(v)); v = fmaxf(v, dpp<XOR8>(v));
  auto s = __builtin_amdgcn_permlane16_swap(asu(v), asu(v), false, false);
  v = fmaxf(asf(s[0]), asf(s[1]));
  auto t = __builtin_amdgcn_permlane32_swap(asu(v), asu(v), false, false);
  return fmaxf(asf(t[0]), asf(t[1]));
}
__device__ __forceinline__ float reduce8(const float (&x)[8], int lane) {
  const bool b3 = lane & 8, b2 = lane & 4;
  float y[4];
#pragma unroll
  for (int i = 0; i < 4; ++i) y[i] = swap16_add(x[i], x[i + 4]);
  float z[2];
#pragma unroll
  for (int i = 0; i < 2; ++i) { const float t = b3 ? y[i + 2] : y[i], u = b3 ? y[i] : y[i + 2]; z[i] = t + dpp<XOR8>(u); }
  const float t = b2 ? z[1] : z[0], u = b2 ? z[0] : z[1];
  float w = t + dpp<XOR7>(u);
  w += dpp<XOR2>(w);
  return w + dpp<XOR1>(w);
}
__device__ __forceinline__ int reduce8_index(int lane) { return ((lane >> 4) & 1) * 4 + ((lane >> 3) & 1) * 2 + ((lane >> 2) & 1); }
__device__ __forceinline__ float reduce4(float a, float b, float c, float d) { return swap32_add(swap16_add(a, b), swap16_add(c, d)); }
__device__ __forceinline__ float ln_rstd(float var) { return __builtin_amdgcn_rcpf(bf16r(__builtin_amdgcn_sqrtf(bf16r(bf16r(var) + 1e-5f)))); }

__device__ __forceinline__ u64 next_tag(v2u e) { return (((u64)e[1] << 32) | e[0]) * 6364136223846793005ull + 1442695040888963407ull; }
__device__ __forceinline__ unsigned fill(float v) { return min(asu(v), EMPTY - 1); }
__device__ __forceinline__ v4u fill4(v4f v) { return v4u{fill(v[0]), fill(v[1]), fill(v[2]), fill(v[3])}; }
__device__ __forceinline__ bool missing(v4u v) {
  return __builtin_amdgcn_ballot_w64(max(max(max(v[0], v[1]), v[2]), v[3]) == EMPTY) != 0;
}
__device__ __forceinline__ v4u ld128(rsrc_t rs, int off) { return __builtin_amdgcn_raw_buffer_load_b128(rs, off, 0, AUX_SC1); }
__device__ __forceinline__ v2u ld64(rsrc_t rs, int off) { return __builtin_amdgcn_raw_buffer_load_b64(rs, off, 0, AUX_SC1); }
__device__ __forceinline__ unsigned ld32(rsrc_t rs, int off) { return __builtin_amdgcn_raw_buffer_load_b32(rs, off, 0, AUX_SC1); }
__device__ __forceinline__ void st128(rsrc_t rs, int off, v4u v) { __builtin_amdgcn_raw_buffer_store_b128(v, rs, off, 0, AUX_SC1); }
__device__ __forceinline__ void st64(rsrc_t rs, int off, v2u v) { __builtin_amdgcn_raw_buffer_store_b64(v, rs, off, 0, AUX_SC1); }
__device__ __forceinline__ void st32(rsrc_t rs, int off, unsigned v) { __builtin_amdgcn_raw_buffer_store_b32(v, rs, off, 0, AUX_SC1); }
constexpr int OOB = 0x7FFFFFF0;

template <typename T> __device__ __forceinline__ T lds_ld(lptr p) { return *(const LAS T*)p; }
template <typename T> __device__ __forceinline__ void lds_st(lptr p, T v) { *(LAS T*)p = v; }
__device__ __forceinline__ unsigned lds_poll(lptr p) { return *(const volatile LAS unsigned*)p; }
__device__ __forceinline__ void lds_post(lptr p, unsigned v) { *(volatile LAS unsigned*)p = v; }
template <typename T> __device__ __forceinline__ T ld_nt(const void* p) { return __builtin_nontemporal_load((const T*)p); }

template <int WIN> __device__ __forceinline__ void dma4(unsigned voff, const char* gp, unsigned ldsaddr) {
  asm volatile("s_mov_b32 m0, %2\n\ts_nop 0\n\t"
               "s_waitcnt vmcnt(%3)\n\tglobal_load_lds_dwordx4 %0, %1 nt\n\t"
               "s_waitcnt vmcnt(%3)\n\tglobal_load_lds_dwordx4 %0, %1 offset:1024 nt\n\t"
               "s_waitcnt vmcnt(%3)\n\tglobal_load_lds_dwordx4 %0, %1 offset:2048 nt\n\t"
               "s_waitcnt vmcnt(%3)\n\tglobal_load_lds_dwordx4 %0, %1 offset:3072 nt"
               :: "v"(voff), "s"(gp), "s"(ldsaddr), "i"(WIN - 1) : "memory");
}

struct Args {
  const u16* x; const char* Kc; const char* Vc; const u16* wq; const u16* wo;
  const u16* w1; const u16* b1; const u16* w2; const u16* b2; u16* out; char* ws;
};

__device__ __forceinline__ void loader_wave(const Args& a, lptr L, int lane, int l, int b, unsigned t0) {
  __builtin_amdgcn_s_setprio(3);
  const char* Kp = a.Kc + ((size_t)b << 20);
  const char* Vp = a.Vc + ((size_t)b << 20) - (size_t)128 * 8192;
  const unsigned voff = lane * 16;
  const unsigned ring = (unsigned)(size_t)(L + L_RING);
  const lptr land = L + L_LAND + l * 4;
  constexpr int NTL = 256 / NL, KLAG = (WIN_MAX - 1) >> 3;
  static_assert(((WIN_MAX - 1) & 7) >= 4, "the landed count is posted behind the second half of a tile");
  BAR0();
  unsigned freed = 0;
  unsigned now = (unsigned)__builtin_amdgcn_s_memrealtime();
  asm volatile("" : "+s"(now));
  for (int k = 1; k < NTL; ++k) {
    const int T = k * NL + l;
    if (T >= 16) {
      const unsigned need = (T - 16) >> 2;
      unsigned f = __builtin_amdgcn_readfirstlane(freed);
      for (unsigned spins = 0; f <= need && spins < LDS_SPIN_CAP; ++spins) f = __builtin_amdgcn_readfirstlane(lds_poll(L + L_FREE + (T & 3) * 4));
    }
    freed = lds_poll(L + L_FREE + ((T + NL) & 3) * 4);
    const int Tp = T - NL;
    int step = (int)(now - ((Tp < 128 ? Tp * TAU_K : 128 * TAU_K + (Tp - 128) * TAU_V) >> 8) - (t0 + SCHED_OFF)) >> LAG_SHIFT;
    asm volatile("" : "+s"(step));
    u64 clock;
    asm volatile("s_memrealtime %0" : "=s"(clock) :: "memory");
    const char* src = (T < 128 ? Kp : Vp) + (size_t)T * 8192;
    const unsigned dst = ring + (T & 15) * 8192;
#define TILE(WIN) { dma4<WIN>(voff, src, dst); dma4<WIN>(voff, src + 4096, dst + 4096); if (lane == 0) lds_post(land, k - KLAG); CBAR(); }
    if (T < START_TILES) TILE(WIN_START)
    else if (step < -1) TILE(WIN_AHEAD)
    else if (step < 0) TILE(WIN_EARLY)
    else if (step < 1) TILE(WIN_ON)
    else if (step < FAR_STEPS) TILE(WIN_LATE)
    else TILE(WIN_FAR)
#undef TILE
    asm volatile("s_waitcnt lgkmcnt(0)" : "+s"(clock) :: "memory");
    now = (unsigned)clock;
  }
#pragma unroll
  for (int k = NTL - 1 - KLAG; k < NTL; ++k) {
    asm volatile("s_waitcnt vmcnt(%0)" :: "i"((NTL - 1 - k) * 8) : "memory");
    if (lane == 0) lds_post(land, k + 1);
    CBAR();
  }
}

__device__ __forceinline__ void consumer_wave(const Args& a, lptr L, int lane, int cw, int b) {
  const int hf = lane >> 5, c = lane & 31;
  if (cw == 0) {
    if (lane < 4) { lds_st<unsigned>(L + L_FAC + lane * 4, EMPTY); lds_st<unsigned>(L + L_LAND + lane * 4, 0u); lds_st<unsigned>(L + L_FREE + lane * 4, 0u); }
    if (lane < 2) lds_st<unsigned>(L + L_CNT + lane * 4, 0u);
  }
  BAR0();
  const lptr rl = L + L_RING + lane * 16;
  const lptr land = L + L_LAND + (cw % NL) * 4, freep = L + L_FREE + cw * 4;
  auto fetch = [&](int n, v4u (&T)[8]) {
    const int t = cw + 4 * n;
    for (unsigned spins = 0; (int)lds_poll(land) <= t / NL && spins < LDS_SPIN_CAP; ++spins) __builtin_amdgcn_s_sleep(1);
    CBAR();
    const lptr p = rl + (t & 15) * 8192;
#pragma unroll
    for (int j = 0; j < 8; ++j) T[j] = lds_ld<v4u>(p + j * 1024);
    asm volatile("s_waitcnt lgkmcnt(0)" : "+v"(T[0]), "+v"(T[1]), "+v"(T[2]), "+v"(T[3]), "+v"(T[4]), "+v"(T[5]), "+v"(T[6]), "+v"(T[7]) :: "memory");
    if (lane == 0) lds_post(freep, n + 1);
    CBAR();
  };
  v4u A[8], B[8], C[8];
  fetch(0, A);
  fetch(1, B);
  fetch(2, C);
  for (unsigned spins = 0; lds_poll(L + L_LAND + 8) == 0 && spins < LDS_SPIN_CAP; ++spins) __builtin_amdgcn_s_sleep(1);
  CBAR();
  const v4u q = lds_ld<v4u>(L + L_Q + c * 16);
  const lptr scw = L + L_SC + cw * 2048;
  const int vidx = reduce8_index(lane);
  auto qk = [&](const v4u (&T)[8], int n) {
    float x[8];
#pragma unroll
    for (int j = 0; j < 8; ++j) { float d = dot2(T[j][0], q[0], 0.f); d = dot2(T[j][1], q[1], d); d = dot2(T[j][2], q[2], d); x[j] = dot2(T[j][3], q[3], d); }
    lds_st<float>(scw + (hf * 256 + n * 8 + vidx) * 4, bf16r(reduce8(x, lane)) * 0.0625f);
  };
  qk(A, 0);
  qk(B, 1);
  qk(C, 2);
  for (int n = 3; n < 32; ++n) { fetch(n, A); qk(A, n); }
  {
    v4f s0 = lds_ld<v4f>(scw + lane * 32), s1 = lds_ld<v4f>(scw + lane * 32 + 16);
    const float m = wave_max(fmaxf(fmaxf(fmaxf(s0[0], s0[1]), fmaxf(s0[2], s0[3])), fmaxf(fmaxf(s1[0], s1[1]), fmaxf(s1[2], s1[3]))));
#pragma unroll
    for (int k = 0; k < 4; ++k) { s0[k] = __builtin_amdgcn_exp2f((s0[k] - m) * L2E); s1[k] = __builtin_amdgcn_exp2f((s1[k] - m) * L2E); }
    const float l = wave_sum(((s0[0] + s0[1]) + (s0[2] + s0[3])) + ((s1[0] + s1[1]) + (s1[2] + s1[3])));
    lds_st<v4u>(scw + lane * 16, v4u{pk_bf16(s0[0], s0[1]), pk_bf16(s0[2], s0[3]), pk_bf16(s1[0], s1[1]), pk_bf16(s1[2], s1[3])});
    if (lane == 0) {
      lds_st<v2f>(L + L_ML + cw * 8, v2f{m, l});
      CBAR();
      __hip_atomic_fetch_add((LAS unsigned*)(L + L_CNT), 1u, __ATOMIC_RELAXED, __HIP_MEMORY_SCOPE_WORKGROUP);
      CBAR();
    }
  }
  float acc[8];
#pragma unroll
  for (int k = 0; k < 8; ++k) acc[k] = 0.f;
  for (int n = 0; n < 32; ++n) {
    const v4u pp = lds_ld<v4u>(scw + (hf * 128 + n * 4) * 4);
    fetch(32 + n, A);
#pragma unroll
    for (int i = 0; i < 4; ++i)
#pragma unroll
      for (int k = 0; k < 4; ++k) {
        acc[2 * k] = dot2(perm_lo(A[2 * i][k], A[2 * i + 1][k]), pp[i], acc[2 * k]);
        acc[2 * k + 1] = dot2(perm_hi(A[2 * i][k], A[2 * i + 1][k]), pp[i], acc[2 * k + 1]);
      }
  }
  float fac = asf(lds_poll(L + L_FAC + cw * 4));
  for (unsigned spins = 0; asu(fac) == EMPTY && spins < LDS_SPIN_CAP; ++spins) { __builtin_amdgcn_s_sleep(1); fac = asf(lds_poll(L + L_FAC + cw * 4)); }
  float o[4];
#pragma unroll
  for (int k = 0; k < 4; ++k) o[k] = swap32_add(acc[k], acc[k + 4]) * fac;
  lds_st<v4f>(L + L_RED + cw * 1024 + c * 32 + hf * 16, v4f{o[0], o[1], o[2], o[3]});
  CBAR();
  unsigned old = 0;
  if (lane == 0) old = __hip_atomic_fetch_add((LAS unsigned*)(L + L_CNT + 4), 1u, __ATOMIC_RELAXED, __HIP_MEMORY_SCOPE_WORKGROUP);
  CBAR();
  if (__builtin_amdgcn_readfirstlane(old) == SW - 1) {
    const v4f sum = (lds_ld<v4f>(L + L_RED + lane * 16) + lds_ld<v4f>(L + L_RED + 1024 + lane * 16)) + (lds_ld<v4f>(L + L_RED + 2048 + lane * 16) + lds_ld<v4f>(L + L_RED + 3072 + lane * 16));
    const rsrc_t ws = __builtin_amdgcn_make_buffer_rsrc((void*)a.ws, 0, WS_BYTES, 0x00020000);
    st128(ws, WS_PO + b * 1024 + lane * 16, fill4(sum));
  }
}

__global__ void __launch_bounds__(NTHR, 4) fused_kernel(Args a) {
  extern __shared__ __attribute__((aligned(16))) char smem[];
  const lptr L = (lptr)smem;
  const int lane = threadIdx.x & 63, wave = __builtin_amdgcn_readfirstlane(threadIdx.x >> 6);
  const int hf = lane >> 5, c = lane & 31;
  const int b = (blockIdx.x & 7) * 32 + (blockIdx.x >> 3);
  const int h = b >> 4, s = b & 15;
  const unsigned t0 = (unsigned)__builtin_amdgcn_s_memrealtime();
  {
    const char* src = a.Kc + ((size_t)b << 20) + (size_t)wave * 8192;
    const unsigned dst = (unsigned)(size_t)(L + L_RING) + wave * 8192;
    asm volatile("s_cmp_lt_u32 %5, %6\n\ts_cbranch_scc0 .Lfirst%=\n\t"
                 "s_mov_b32 m0, %2\n\ts_nop 0\n\t"
                 "global_load_lds_dwordx4 %0, %1 nt\n\tglobal_load_lds_dwordx4 %0, %1 offset:1024 nt\n\t"
                 "global_load_lds_dwordx4 %0, %1 offset:2048 nt\n\tglobal_load_lds_dwordx4 %0, %1 offset:3072 nt\n\t"
                 "s_mov_b32 m0, %4\n\ts_nop 0\n\t"
                 "global_load_lds_dwordx4 %0, %3 nt\n\tglobal_load_lds_dwordx4 %0, %3 offset:1024 nt\n\t"
                 "global_load_lds_dwordx4 %0, %3 offset:2048 nt\n\tglobal_load_lds_dwordx4 %0, %3 offset:3072 nt\n"
                 ".Lfirst%=:"
                 :: "v"(lane * 16), "s"(src), "s"(dst), "s"(src + 4096), "s"(dst + 4096), "s"(wave), "i"(NL) : "memory", "scc");
  }
  if (wave < NL) { loader_wave(a, L, lane, wave, b, t0); return; }
  if (wave < NL + SW) { consumer_wave(a, L, lane, wave - NL, b); return; }

  const int tw = wave - NL - SW;
  const int jm = s < 2 ? h + 16 * s : NMLP;
  const bool is_mlp = jm < NMLP;
  const rsrc_t ws = __builtin_amdgcn_make_buffer_rsrc((void*)a.ws, 0, WS_BYTES, 0x00020000);
  if (tw != 0) BAR0();
  if (tw == 0) {
    const v4u xraw = *(const v4u*)(a.x + min(lane, 55) * 8);
    v4u wv[14];
    const u16* wp = a.wq + (size_t)c * (H * DH) + h * DH + 16 * s + 8 * hf;
#pragma unroll
    for (int i = 0; i < 14; ++i) wv[i] = ld_nt<v4u>(wp + (size_t)32 * i * (H * DH));
    BAR0();
    const v2u e = ld64(ws, WS_EPOCH);
    float xv[8];
#pragma unroll
    for (int k = 0; k < 4; ++k) { xv[2 * k] = lo16(xraw[k]); xv[2 * k + 1] = hi16(xraw[k]); }
    const bool act = lane < 56;
    float sm = 0.f;
#pragma unroll
    for (int k = 0; k < 8; ++k) sm += xv[k];
    const float mean = wave_sum(act ? sm : 0.f) * (1.f / D);
    float v = 0.f;
#pragma unroll
    for (int k = 0; k < 8; ++k) { const float d = xv[k] - mean; v = fmaf(d, d, v); }
    const float mb = bf16r(mean), rs = ln_rstd(wave_sum(act ? v : 0.f) * (1.f / D));
    if (act) {
      lds_st<v2u>(L + L_H + lane * 16, v2u{pk_bf16(bf16r(xv[0] - mb) * rs, bf16r(xv[1] - mb) * rs), pk_bf16(bf16r(xv[2] - mb) * rs, bf16r(xv[3] - mb) * rs)});
      lds_st<v2u>(L + L_H + lane * 16 + 8, v2u{pk_bf16(bf16r(xv[4] - mb) * rs, bf16r(xv[5] - mb) * rs), pk_bf16(bf16r(xv[6] - mb) * rs, bf16r(xv[7] - mb) * rs)});
    }
    float qa[8];
#pragma unroll
    for (int k = 0; k < 8; ++k) qa[k] = 0.f;
#pragma unroll
    for (int t = 0; t < 7; ++t) {
      const unsigned xp = (unsigned)lds_ld<u16>(L + L_H + (c + 64 * t) * 2) | ((unsigned)lds_ld<u16>(L + L_H + (c + 64 * t + 32) * 2) << 16);
#pragma unroll
      for (int k = 0; k < 4; ++k) {
        qa[2 * k] = dot2(perm_lo(wv[2 * t][k], wv[2 * t + 1][k]), xp, qa[2 * k]);
        qa[2 * k + 1] = dot2(perm_hi(wv[2 * t][k], wv[2 * t + 1][k]), xp, qa[2 * k + 1]);
      }
    }
    const float qv = bf16r(reduce8(qa, lane));
    const u64 tag = next_tag(e);
    const unsigned tlo = (unsigned)(tag >> 16) << 16, thi = (unsigned)(tag >> 32);
    if ((lane & 3) == 0) st64(ws, WS_QB + h * 2048 + (16 * s + 8 * hf + reduce8_index(lane)) * 8, v2u{tlo | (asu(qv) >> 16), thi});
    v4u g0 = {0, 0, 0, 0}, g1 = {0, 0, 0, 0};
    bool have = false;
    for (unsigned spins = 0; spins < SPIN_CAP; ++spins) {
      CBAR();
      const int off = have ? OOB : WS_QB + h * 2048 + lane * 32;
      const v4u n0 = ld128(ws, off), n1 = ld128(ws, off + 16);
      if (!have) { g0 = n0; g1 = n1; }
      have = (g0[0] & 0xffff0000u) == tlo && g0[1] == thi && (g0[2] & 0xffff0000u) == tlo && g0[3] == thi &&
             (g1[0] & 0xffff0000u) == tlo && g1[1] == thi && (g1[2] & 0xffff0000u) == tlo && g1[3] == thi;
      if (__all(have)) break;
      __builtin_amdgcn_s_sleep(4);
    }
    lds_st<v2u>(L + L_Q + lane * 8, v2u{perm_lo(g0[0], g0[2]), perm_lo(g1[0], g1[2])});
    CBAR();
    if (lane == 0) lds_post(L + L_LAND + 8, 1u);
  }
  if (tw == 1) {
    const v2u e = ld64(ws, WS_EPOCH);
    const v4u none = {EMPTY, EMPTY, EMPTY, EMPTY};
    st128(ws, WS_PO + b * 1024 + lane * 16, none);
    if (lane < 8) st128(ws, WS_Y + b * 128 + lane * 16, none);
    if (lane >= 8 && lane < 16) st128(ws, WS_PML + b * 128 + (lane - 8) * 16, none);
    if (is_mlp) { st128(ws, WS_OP + jm * 1792 + lane * 16, none); if (lane < 48) st128(ws, WS_OP + jm * 1792 + 1024 + lane * 16, none); }
    DRAIN();
    const u64 tag = next_tag(e);
    if (lane == 0) st64(ws, WS_FLAG + b * 8, v2u{(unsigned)tag, (unsigned)(tag >> 32)});
  }
  __builtin_amdgcn_s_sleep(127);
  __builtin_amdgcn_s_sleep(127);
  if (tw == 3) {
    const u64 tg = next_tag(ld64(ws, WS_EPOCH));
    const unsigned tl = (unsigned)tg, th = (unsigned)(tg >> 32);
    const bool mine = is_mlp || (lane >> 2) == h;
    for (unsigned spins = 0; spins < SPIN_CAP; ++spins) {
      CBAR();
      const v4u f0 = ld128(ws, mine ? WS_FLAG + lane * 32 : OOB), f1 = ld128(ws, mine ? WS_FLAG + lane * 32 + 16 : OOB);
      const bool ok = f0[0] == tl && f0[1] == th && f0[2] == tl && f0[3] == th && f1[0] == tl && f1[1] == th && f1[2] == tl && f1[3] == th;
      if (__all(ok || !mine)) break;
      __builtin_amdgcn_s_sleep(64);
    }
  }
  if (tw == 4) {
    const unsigned w0 = ld_nt<unsigned>(a.Vc + ((size_t)b << 20) + (size_t)(lane & 1) * 524288);
    asm volatile("" :: "v"(w0));
  }
  v2u woraw[4];
  {
    const u16* p = a.wo + (size_t)(h * 256 + 32 * tw + 4 * (lane & 7)) * D + 28 * s + 4 * min(lane >> 3, 6);
#pragma unroll
    for (int r = 0; r < 4; ++r) { woraw[r] = ld_nt<v2u>(p + r * D); PACE(); }
  }
  v2u w1raw[14]; v4u w2raw[8]; unsigned xraw1 = 0, b2raw = 0, b1raw = 0;
  const int col4 = (lane & 15) * 4 + ((lane >> 4) & 1) + 2 * (lane >> 5);
  if (is_mlp) {
    const u16* p1 = a.w1 + (size_t)(56 * tw + 14 * (lane >> 4)) * F + 64 * jm + 4 * (lane & 15);
#pragma unroll
    for (int r = 0; r < 14; ++r) { w1raw[r] = ld_nt<v2u>(p1 + (size_t)r * F); PACE(); }
    const u16* p2 = a.w2 + (size_t)(64 * jm + 8 * tw) * D + 8 * min(lane, 55);
#pragma unroll
    for (int r = 0; r < 8; ++r) { w2raw[r] = ld_nt<v4u>(p2 + r * D); PACE(); }
    xraw1 = a.x[64 * min(tw, 6) + col4];
    b2raw = a.b2[64 * min(jm, 6) + col4];
    b1raw = a.b1[64 * jm + 8 * tw + (lane & 7)];
  } else {
#pragma unroll
    for (int r = 0; r < 14; ++r) w1raw[r] = v2u{0, 0};
#pragma unroll
    for (int r = 0; r < 8; ++r) w2raw[r] = v4u{0, 0, 0, 0};
  }
  unsigned wop[8], w1p[28], w2p[32];
#pragma unroll
  for (int t = 0; t < 2; ++t)
#pragma unroll
    for (int k = 0; k < 2; ++k) { wop[t * 4 + 2 * k] = perm_lo(woraw[2 * t][k], woraw[2 * t + 1][k]); wop[t * 4 + 2 * k + 1] = perm_hi(woraw[2 * t][k], woraw[2 * t + 1][k]); }
#pragma unroll
  for (int t = 0; t < 7; ++t)
#pragma unroll
    for (int k = 0; k < 2; ++k) { w1p[t * 4 + 2 * k] = perm_lo(w1raw[2 * t][k], w1raw[2 * t + 1][k]); w1p[t * 4 + 2 * k + 1] = perm_hi(w1raw[2 * t][k], w1raw[2 * t + 1][k]); }
#pragma unroll
  for (int t = 0; t < 4; ++t)
#pragma unroll
    for (int k = 0; k < 4; ++k) { w2p[t * 8 + 2 * k] = perm_lo(w2raw[2 * t][k], w2raw[2 * t + 1][k]); w2p[t * 8 + 2 * k + 1] = perm_hi(w2raw[2 * t][k], w2raw[2 * t + 1][k]); }
  float csum;
  {
    float y[4] = {0.f, 0.f, 0.f, 0.f};
#pragma unroll
    for (int t = 0; t < 7; ++t)
#pragma unroll
      for (int k = 0; k < 4; ++k) y[k] = dot2(w1p[t * 4 + k], 0x3F803F80u, y[k]);
    csum = reduce4(y[0], y[1], y[2], y[3]);
  }
  const float xres = asf(xraw1 << 16), b1v = asf(b1raw << 16), b2v = asf(b2raw << 16);
  asm volatile("" : "+v"(csum));
#pragma unroll
  for (int i = 0; i < 8; ++i) asm volatile("" : "+v"(wop[i]));
#pragma unroll
  for (int i = 0; i < 28; ++i) asm volatile("" : "+v"(w1p[i]));
#pragma unroll
  for (int i = 0; i < 32; ++i) asm volatile("" : "+v"(w2p[i]));
  u64 tagf = 0;
  if (jm == 0 && tw == 7) { tagf = next_tag(ld64(ws, WS_EPOCH)); asm volatile("" : "+v"(tagf)); }

  if (tw == 3) {
    for (unsigned spins = 0; lds_poll(L + L_CNT) < SW && spins < LDS_SPIN_CAP; ++spins) __builtin_amdgcn_s_sleep(8);
    CBAR();
    const v2f ml = lds_ld<v2f>(L + L_ML + (lane & 3) * 8);
    float Mw = fmaxf(ml[0], dpp<XOR1>(ml[0])); Mw = fmaxf(Mw, dpp<XOR2>(Mw));
    float Lw = __builtin_amdgcn_exp2f((ml[0] - Mw) * L2E) * ml[1]; Lw += dpp<XOR1>(Lw); Lw += dpp<XOR2>(Lw);
    if (lane < 8) st128(ws, WS_PML + b * 128 + lane * 16, v4u{fill(Mw), fill(Lw), 0u, 0u});
    v2u pm;
    for (unsigned spins = 0; spins < SPIN_CAP; ++spins) {
      __builtin_amdgcn_s_sleep(100);
      CBAR();
      pm = ld64(ws, lane < 16 ? WS_PML + (h * 16 + lane) * 128 : OOB);
      if (__all(pm[0] != EMPTY && pm[1] != EMPTY)) break;
    }
    float M = fmaxf(asf(pm[0]), dpp<XOR1>(asf(pm[0]))); M = fmaxf(M, dpp<XOR2>(M)); M = fmaxf(M, dpp<XOR7>(M)); M = fmaxf(M, dpp<XOR8>(M));
    float Ls = __builtin_amdgcn_exp2f((asf(pm[0]) - M) * L2E) * asf(pm[1]); Ls += dpp<XOR1>(Ls); Ls += dpp<XOR2>(Ls); Ls += dpp<XOR7>(Ls); Ls += dpp<XOR8>(Ls);
    if (lane < 4) lds_st<unsigned>(L + L_FAC + lane * 4, fill(__builtin_amdgcn_exp2f((ml[0] - M) * L2E) / Ls));
    for (unsigned spins = 0; (int)lds_poll(L + L_LAND) < 256 / NL - 2 && spins < LDS_SPIN_CAP; ++spins) __builtin_amdgcn_s_sleep(16);
    for (unsigned spins = 0; spins < (SPIN_CAP << 2); ++spins) {
      CBAR();
      const unsigned ps = ld32(ws, lane < 16 ? WS_PO + (h * 16 + lane) * 1024 : OOB);
      if (__builtin_popcountll(__ballot(ps == EMPTY && lane != s)) <= 2) break;
      __builtin_amdgcn_s_sleep(4);
    }
  }
  __syncthreads();
  {
    const int sub = lane >> 3, quad = lane & 7;
    const int off = WS_PO + (h * 16 + sub) * 1024 + tw * 128 + quad * 16;
    v4u pa = ld128(ws, off), pb = ld128(ws, off + 8192);
    for (unsigned spins = 0; spins < SPIN_CAP; ++spins) {
      const bool ma = missing(pa), mb = missing(pb);
      if (!(ma || mb)) break;
      if (spins >= 2) __builtin_amdgcn_s_sleep(4);
      CBAR();
      if (ma) pa = ld128(ws, off);
      if (mb) pb = ld128(ws, off + 8192);
    }
    const v4f o = asf4(pa) + asf4(pb);
    const unsigned o01 = pk_bf16(sum_hi3(o[0]), sum_hi3(o[1])), o23 = pk_bf16(sum_hi3(o[2]), sum_hi3(o[3]));
    v4f y;
#pragma unroll
    for (int k = 0; k < 4; ++k) y[k] = sum_lo3(dot2(wop[4 + k], o23, dot2(wop[k], o01, 0.f)));
    if (quad == 0) lds_st<v4f>(L + L_Y + tw * 128 + sub * 16, y);
  }
  __syncthreads();
  if (tw == 1) {
    const lptr p = L + L_Y + hf * 512 + c * 4;
    float y = (lds_ld<float>(p) + lds_ld<float>(p + 128)) + (lds_ld<float>(p + 256) + lds_ld<float>(p + 384));
    y = swap32_add(y, y);
    if (lane < 32) st32(ws, WS_Y + b * 128 + lane * 4, lane < 28 ? fill(y) : 0u);
  }
  if (!is_mlp) return;
  if (tw < 7) {
    const int qd = tw * 16 + (lane & 15);
    const int off = WS_Y + (lane >> 4) * 2048 + (qd / 7) * 128 + (qd % 7) * 16;
    v4u y0 = ld128(ws, off), y1 = ld128(ws, off + 4 * 2048), y2 = ld128(ws, off + 8 * 2048), y3 = ld128(ws, off + 12 * 2048);
    for (unsigned spins = 0; spins < SPIN_CAP; ++spins) {
      const bool m0 = missing(y0), m1 = missing(y1), m2 = missing(y2), m3 = missing(y3);
      if (!(m0 || m1 || m2 || m3)) break;
      if (spins >= 2) __builtin_amdgcn_s_sleep(4);
      CBAR();
      if (m0) y0 = ld128(ws, off);
      if (m1) y1 = ld128(ws, off + 4 * 2048);
      if (m2) y2 = ld128(ws, off + 8 * 2048);
      if (m3) y3 = ld128(ws, off + 12 * 2048);
    }
    const v4f ys = (asf4(y0) + asf4(y1)) + (asf4(y2) + asf4(y3));
    const float at = reduce4(ys[0], ys[1], ys[2], ys[3]);
    lds_st<u16>(L + L_H + (tw * 64 + col4) * 2, (u16)(pk_bf16(xres + bf16r(at), 0.f) & 0xffffu));
  }
  __syncthreads();
  if (jm == 0 && tw == 7 && lane == 0) st64(ws, WS_EPOCH, v2u{(unsigned)tagf, (unsigned)(tagf >> 32)});
  float rs;
  {
    const v4u hq = lds_ld<v4u>(L + L_H + min(lane, 55) * 16);
    unsigned hp[7];
#pragma unroll
    for (int t = 0; t < 7; ++t) hp[t] = lds_ld<unsigned>(L + L_H + (56 * tw + 14 * (lane >> 4) + 2 * t) * 2);
    const float c0 = lo16(lds_ld<unsigned>(L + L_H));
    float s1 = 0.f, s2 = 0.f;
#pragma unroll
    for (int k = 0; k < 4; ++k) { const float d0 = lo16(hq[k]) - c0, d1 = hi16(hq[k]) - c0; s1 += d0 + d1; s2 = fmaf(d0, d0, fmaf(d1, d1, s2)); }
    if (lane >= 56) { s1 = 0.f; s2 = 0.f; }
    float y[4] = {0.f, 0.f, 0.f, 0.f};
#pragma unroll
    for (int t = 0; t < 7; ++t)
#pragma unroll
      for (int k = 0; k < 4; ++k) y[k] = dot2(w1p[t * 4 + k], hp[t], y[k]);
    s1 = wave_sum(s1) * (1.f / D); s2 = wave_sum(s2) * (1.f / D);
    const float mb = bf16r(c0 + s1);
    rs = ln_rstd(fmaxf(fmaf(-s1, s1, s2), 0.f));
    lds_st<float>(L + L_H1 + (tw * 64 + col4) * 4, fmaf(-mb, csum, reduce4(y[0], y[1], y[2], y[3])));
  }
  __syncthreads();
  {
    float v = sum_hi3(lds_ld<float>(L + L_H1 + ((lane >> 3) * 64 + 8 * tw + (lane & 7)) * 4)) * rs;
    v = fmaxf(bf16r(bf16r(v) + b1v), 0.f);
    v = v * v;
    const unsigned pr = pk_bf16(v, dpp<XOR1>(v));
    float y[8];
#pragma unroll
    for (int k = 0; k < 8; ++k) y[k] = 0.f;
#pragma unroll
    for (int t = 0; t < 4; ++t) {
      const unsigned hp = __builtin_amdgcn_readlane(pr, 2 * t);
#pragma unroll
      for (int k = 0; k < 8; ++k) y[k] = dot2(w2p[t * 8 + k], hp, y[k]);
    }
    if (lane < 56) {
      lds_st<v4f>(L + L_OP + tw * 1792 + lane * 32, v4f{y[0], y[1], y[2], y[3]});
      lds_st<v4f>(L + L_OP + tw * 1792 + lane * 32 + 16, v4f{y[4], y[5], y[6], y[7]});
    }
  }
  __syncthreads();
  if (tw < 7) {
    const lptr p = L + L_OP + (tw * 64 + lane) * 4;
    const float y = ((lds_ld<float>(p) + lds_ld<float>(p + 1792)) + (lds_ld<float>(p + 2 * 1792) + lds_ld<float>(p + 3 * 1792))) +
                    ((lds_ld<float>(p + 4 * 1792) + lds_ld<float>(p + 5 * 1792)) + (lds_ld<float>(p + 6 * 1792) + lds_ld<float>(p + 7 * 1792)));
    st32(ws, WS_OP + jm * 1792 + (tw * 64 + lane) * 4, fill(y));
  }
  if (jm < NFIN && tw == 7) {
    const int off = WS_OP + (lane >> 4) * 1792 + jm * 256 + (lane & 15) * 16;
    const float hres = asf((unsigned)lds_ld<u16>(L + L_H + (jm * 64 + col4) * 2) << 16);
    v4u p[7];
#pragma unroll
    for (int i = 0; i < 7; ++i) p[i] = ld128(ws, off + i * 4 * 1792);
    for (unsigned spins = 0; spins < SPIN_CAP; ++spins) {
      bool m[7], any = false;
#pragma unroll
      for (int i = 0; i < 7; ++i) { m[i] = missing(p[i]); any = any || m[i]; }
      if (!any) break;
      CBAR();
#pragma unroll
      for (int i = 0; i < 7; ++i) if (m[i]) p[i] = ld128(ws, off + i * 4 * 1792);
    }
    const v4f ys = ((asf4(p[0]) + asf4(p[1])) + (asf4(p[2]) + asf4(p[3]))) + ((asf4(p[4]) + asf4(p[5])) + asf4(p[6]));
    const float t = reduce4(ys[0], ys[1], ys[2], ys[3]);
    const float o = bf16r(hres + bf16r(bf16r(t) + b2v));
    a.out[jm * 64 + col4] = (u16)(asu(o) >> 16);
  }
}
}

extern "C" void kernel_launch(void* const* d_in, const int* in_sizes, int n_in, void* d_out, int out_size, void* d_ws, size_t ws_size, hipStream_t stream) {
  Args a;
  a.x = (const u16*)d_in[0]; a.Kc = (const char*)d_in[1]; a.Vc = (const char*)d_in[2]; a.wq = (const u16*)d_in[3]; a.wo = (const u16*)d_in[4];
  a.w1 = (const u16*)d_in[6]; a.b1 = (const u16*)d_in[7]; a.w2 = (const u16*)d_in[8]; a.b2 = (const u16*)d_in[9];
  a.out = (u16*)d_out; a.ws = (char*)d_ws;
  (void)hipFuncSetAttribute((const void*)fused_kernel, hipFuncAttributeMaxDynamicSharedMemorySize, LDS_BYTES);
  fused_kernel<<<256, NTHR, LDS_BYTES, stream>>>(a);
}
```
